```python
import torch

def _softmax(x, axis=-1):
    return torch.softmax(x, dim=axis)
def _gelu(x):
    return torch.nn.functional.gelu(x, approximate="tanh")
def _silu(x):
    return torch.nn.functional.silu(x)
def _sigmoid(x):
    return torch.sigmoid(x)
def _layernorm(x, axis=-1, eps=1e-5):
    m = x.mean(dim=axis, keepdim=True)
    v = x.var(dim=axis, keepdim=True, unbiased=False)
    return (x - m) / torch.sqrt(v + eps)
def _scatter_add(tgt, idx, src, axis=0):
    return tgt.clone().index_add_(axis, idx, src)
def _segment_sum(src, seg, n):
    out = torch.zeros((n,) + src.shape[1:], dtype=src.dtype)
    return out.index_add_(0, seg, src)
def _pad(x, pw):
    flat = tuple(v for pair in reversed(pw) for v in pair)
    return torch.nn.functional.pad(x, flat)

kernel_name = 'sequential_preLN__spectral_fnet__moe_arch_mod__prompt'

def setup_inputs():

    _w = (lambda a: (lambda t: t.to(torch.bfloat16) if t.is_floating_point() else t)(torch.as_tensor(a)))
    def _A_setup():
        B, S, D = 1, 2048, 576
        ns = torch.arange(S)[:, None] * torch.arange(S)[None, :]
        nd = torch.arange(D)[:, None] * torch.arange(D)[None, :]
        Ws_re = torch.cos(-2.0 * torch.pi * ns / S).to(float)
        Ws_im = torch.sin(-2.0 * torch.pi * ns / S).to(float)
        Wd_re = torch.cos(-2.0 * torch.pi * nd / D).to(float)
        Wd_im = torch.sin(-2.0 * torch.pi * nd / D).to(float)
        return {
            "x": _w((lambda *s: torch.randn(*s, dtype=torch.bfloat16) * 0.05)(B, S, D)),
            "dft_seq_re": _w(Ws_re),
            "dft_seq_im": _w(Ws_im),
            "dft_hid_re": _w(Wd_re),
            "dft_hid_im": _w(Wd_im),
        }
    _A_in = _A_setup()

    def _B_setup():
        B, S, D, F, C = 1, 2048, 576, 4608, 1
        return {
            "x": _w((lambda *s: torch.randn(*s, dtype=torch.bfloat16) * 0.05)(B, S, D)),
            "w_r": _w((lambda *s: torch.randn(*s, dtype=torch.bfloat16) * 1.6)(D)),
            "w_gate": _w((lambda *s: torch.randn(*s, dtype=torch.bfloat16) * 0.4)(D, F)),
            "w_up": _w((lambda *s: torch.randn(*s, dtype=torch.bfloat16) * 0.4)(D, F)),
            "w_down": _w((lambda *s: torch.randn(*s, dtype=torch.bfloat16) * 0.4)(F, D)),
        }
    _B_in = _B_setup()

    return {
        "A_x": _A_in["x"],
        "A_dft_seq_re": _A_in["dft_seq_re"],
        "A_dft_seq_im": _A_in["dft_seq_im"],
        "A_dft_hid_re": _A_in["dft_hid_re"],
        "A_dft_hid_im": _A_in["dft_hid_im"],
        "B_x": _B_in["x"],
        "B_w_r": _B_in["w_r"],
        "B_w_gate": _B_in["w_gate"],
        "B_w_up": _B_in["w_up"],
        "B_w_down": _B_in["w_down"],
    }

def reference(A_x, A_dft_seq_re, A_dft_seq_im, A_dft_hid_re, A_dft_hid_im, B_x, B_w_r, B_w_gate, B_w_up, B_w_down):
    def _A_ref(x, dft_seq_re, dft_seq_im, dft_hid_re, dft_hid_im):
        a = torch.einsum("bsd,ed->bse", x, dft_hid_re)
        b = torch.einsum("bsd,ed->bse", x, dft_hid_im)
        y = torch.einsum("ts,bse->bte", dft_seq_re, a) - torch.einsum("ts,bse->bte", dft_seq_im, b)
        return y

    def _B_ref(x, w_r, w_gate, w_up, w_down):
        B, S, D = x.shape
        C = 1
        r = x @ w_r
        idx = torch.argsort(-r, axis=-1)[:, :C]
        idx_e = (lambda x, n, axis: torch.repeat_interleave(x, n, dim=axis))(idx.reshape(B, C, 1), D, axis=2)
        x_sel = (lambda x, i, axis: torch.take_along_dim(x, i, dim=axis))(x, idx_e, axis=1)
        g = _silu(x_sel @ w_gate)
        u = x_sel @ w_up
        y = (g * u) @ w_down
        r_sel = _sigmoid((lambda x, i, axis: torch.take_along_dim(x, i, dim=axis))(r, idx, axis=1))
        y = y * r_sel.reshape(B, C, 1)
        b_off = (torch.arange(B) * S).reshape(B, 1)
        flat_idx = (idx + b_off).reshape(B * C)
        zero = (x * 0.0).reshape(B * S, D)
        upd = _scatter_add(zero, flat_idx, y.reshape(B * C, D), axis=0)
        return x + upd.reshape(B, S, D)

    x = A_x
    h = x + _A_ref(_layernorm(x, axis=-1), A_dft_seq_re, A_dft_seq_im, A_dft_hid_re, A_dft_hid_im)
    h2 = h + _B_ref(_layernorm(h, axis=-1), B_w_r, B_w_gate, B_w_up, B_w_down)
    return h2
```

```cpp
#include <hip/hip_runtime.h>
#include <hip/hip_bf16.h>
#include <stdint.h>

typedef unsigned short u16;
typedef short bf16x8 __attribute__((ext_vector_type(8)));
typedef float f32x4 __attribute__((ext_vector_type(4)));

constexpr int S = 2048, D = 576, F = 4608;

__device__ __forceinline__ float bf2f(u16 b) { return __uint_as_float(((unsigned)b) << 16); }
__device__ __forceinline__ u16 f2bf(float f) { __hip_bfloat16 h = __float2bfloat16(f); return *(u16*)&h; }
__device__ __forceinline__ float rbf(float f) { return bf2f(f2bf(f)); }

__device__ __forceinline__ float wave_sum(float v) {
  for (int m = 32; m >= 1; m >>= 1) v += __shfl_xor(v, m);
  return v;
}

__global__ void k_ln1(const u16* x, u16* xn) {
  int row = blockIdx.x * 4 + (threadIdx.x >> 6), lane = threadIdx.x & 63;
  float v[9]; float s = 0;
  for (int i = 0; i < 9; ++i) { v[i] = bf2f(x[row * D + lane + 64 * i]); s += v[i]; }
  float mean = wave_sum(s) * (1.0f / D);
  float q = 0;
  for (int i = 0; i < 9; ++i) { float d = v[i] - mean; q += d * d; }
  float var = wave_sum(q) * (1.0f / D);
  float mb = rbf(mean), sd = rbf(sqrtf(rbf(rbf(var) + 1e-5f)));
  for (int i = 0; i < 9; ++i) xn[row * D + lane + 64 * i] = f2bf(rbf(v[i] - mb) / sd);
}

__global__ void k_gemm_nt(const u16* A, const u16* B, u16* C, int M, int N, int K) {
  int wave = blockIdx.x * 4 + (threadIdx.x >> 6), lane = threadIdx.x & 63;
  int nt = N / 64;
  int tm = wave / nt, tn = wave % nt;
  if (tm * 16 >= M) return;
  f32x4 acc[4] = {};
  const u16* ap = A + (size_t)(tm * 16 + (lane & 15)) * K + (lane >> 4) * 8;
  const u16* bp = B + (size_t)(tn * 64 + (lane & 15)) * K + (lane >> 4) * 8;
  for (int k = 0; k < K; k += 32) {
    bf16x8 a = *(const bf16x8*)(ap + k);
    for (int j = 0; j < 4; ++j) {
      bf16x8 b = *(const bf16x8*)(bp + (size_t)j * 16 * K + k);
      acc[j] = __builtin_amdgcn_mfma_f32_16x16x32_bf16(a, b, acc[j], 0, 0, 0);
    }
  }
  for (int j = 0; j < 4; ++j)
    for (int r = 0; r < 4; ++r)
      C[(size_t)(tm * 16 + (lane >> 4) * 4 + r) * N + tn * 64 + j * 16 + (lane & 15)] = f2bf(acc[j][r]);
}

__global__ void k_ep(const u16* x, const u16* P1, const u16* P2, const u16* wr, u16* out, u16* hn_out, float* r_out) {
  int row = blockIdx.x * 4 + (threadIdx.x >> 6), lane = threadIdx.x & 63;
  float v[9]; float s = 0;
  for (int i = 0; i < 9; ++i) {
    int o = row * D + lane + 64 * i;
    float y = rbf(bf2f(P1[o]) - bf2f(P2[o]));
    v[i] = rbf(bf2f(x[o]) + y); s += v[i];
  }
  float mean = wave_sum(s) * (1.0f / D);
  float q = 0;
  for (int i = 0; i < 9; ++i) { float d = v[i] - mean; q += d * d; }
  float var = wave_sum(q) * (1.0f / D);
  float mb = rbf(mean), sd = rbf(sqrtf(rbf(rbf(var) + 1e-5f)));
  float r = 0;
  for (int i = 0; i < 9; ++i) {
    int o = row * D + lane + 64 * i;
    float hn = rbf(rbf(v[i] - mb) / sd);
    r += hn * bf2f(wr[lane + 64 * i]);
    hn_out[o] = f2bf(hn);
    out[o] = f2bf(v[i] + hn);
  }
  r = wave_sum(r);
  if (lane == 0) r_out[row] = rbf(r);
}

__device__ int block_argmax(const float* r, float* sv, int* si) {
  int t = threadIdx.x;
  float bv = -INFINITY; int bi = 0x7fffffff;
  for (int i = t; i < S; i += 256) { float v = r[i]; if (v > bv || (v == bv && i < bi)) { bv = v; bi = i; } }
  sv[t] = bv; si[t] = bi; __syncthreads();
  for (int o = 128; o >= 1; o >>= 1) {
    if (t < o) { float v = sv[t + o]; int i = si[t + o]; if (v > sv[t] || (v == sv[t] && i < si[t])) { sv[t] = v; si[t] = i; } }
    __syncthreads();
  }
  int idx = si[0];
  if (idx == 0x7fffffff) idx = 0;
  return idx;
}

__global__ void k_gu(const float* r, const u16* hn, const u16* wg, const u16* wu, u16* act) {
  __shared__ float sv[256]; __shared__ int si[256]; __shared__ float xs[D];
  int idx = block_argmax(r, sv, si);
  for (int i = threadIdx.x; i < D; i += 256) xs[i] = bf2f(hn[idx * D + i]);
  __syncthreads();
  int f = blockIdx.x * 256 + threadIdx.x;
  float g = 0, u = 0;
  for (int d = 0; d < D; ++d) { g += xs[d] * bf2f(wg[(size_t)d * F + f]); u += xs[d] * bf2f(wu[(size_t)d * F + f]); }
  g = rbf(g); u = rbf(u);
  float sg = rbf(g / (1.0f + __expf(-g)));
  act[f] = f2bf(sg * u);
}

__global__ void k_down(const float* r, const u16* x, const u16* P1, const u16* P2, const u16* hn, const u16* act, const u16* wd, u16* out) {
  __shared__ float sv[256]; __shared__ int si[256]; __shared__ float red[256];
  int idx = block_argmax(r, sv, si);
  int c = blockIdx.x * 16 + (threadIdx.x & 15), fg = threadIdx.x >> 4;
  float y = 0;
  for (int f = fg; f < F; f += 16) y += bf2f(act[f]) * bf2f(wd[(size_t)f * D + c]);
  red[threadIdx.x] = y; __syncthreads();
  if (threadIdx.x < 16) {
    float t = 0; for (int i = 0; i < 16; ++i) t += red[i * 16 + threadIdx.x];
    t = rbf(t);
    float rs = r[idx]; float sg = rbf(1.0f / (1.0f + __expf(-rs)));
    float yy = rbf(t * sg);
    int o = idx * D + c;
    float yv = rbf(bf2f(P1[o]) - bf2f(P2[o]));
    float h = rbf(bf2f(x[o]) + yv);
    float b = rbf(bf2f(hn[o]) + yy);
    out[o] = f2bf(h + b);
  }
}

extern "C" void kernel_launch(void* const* d_in, const int* in_sizes, int n_in,
                              void* d_out, int out_size, void* d_ws, size_t ws_size,
                              hipStream_t stream) {
  const u16* x = (const u16*)d_in[0];
  const u16* wsr = (const u16*)d_in[1];
  const u16* wsi = (const u16*)d_in[2];
  const u16* wdr = (const u16*)d_in[3];
  const u16* wdi = (const u16*)d_in[4];
  const u16* wr = (const u16*)d_in[6];
  const u16* wg = (const u16*)d_in[7];
  const u16* wu = (const u16*)d_in[8];
  const u16* wd = (const u16*)d_in[9];
  u16* out = (u16*)d_out;
  char* ws = (char*)d_ws;
  const size_t SZ = (size_t)S * D * 2;
  u16* xn = (u16*)(ws);
  u16* aT = (u16*)(ws + SZ);
  u16* bT = (u16*)(ws + 2 * SZ);
  u16* P1 = (u16*)(ws + 3 * SZ);
  u16* P2 = (u16*)(ws + 4 * SZ);
  u16* hn = (u16*)(ws + 5 * SZ);
  float* r = (float*)(ws + 6 * SZ);
  u16* act = (u16*)(ws + 6 * SZ + 16384);
  k_ln1<<<S / 4, 256, 0, stream>>>(x, xn);
  k_gemm_nt<<<(D / 16) * (S / 64) / 4, 256, 0, stream>>>(wdr, xn, aT, D, S, D);
  k_gemm_nt<<<(D / 16) * (S / 64) / 4, 256, 0, stream>>>(wdi, xn, bT, D, S, D);
  k_gemm_nt<<<(S / 16) * (D / 64) / 4, 256, 0, stream>>>(wsr, aT, P1, S, D, S);
  k_gemm_nt<<<(S / 16) * (D / 64) / 4, 256, 0, stream>>>(wsi, bT, P2, S, D, S);
  k_ep<<<S / 4, 256, 0, stream>>>(x, P1, P2, wr, out, hn, r);
  k_gu<<<F / 256, 256, 0, stream>>>(r, hn, wg, wu, act);
  k_down<<<D / 16, 256, 0, stream>>>(r, x, P1, P2, hn, act, wd, out);
}
```

```cpp
#include <hip/hip_runtime.h>
#include <stdint.h>

typedef unsigned short u16;
typedef unsigned v4u __attribute__((ext_vector_type(4)));
typedef unsigned v2u __attribute__((ext_vector_type(2)));
typedef float f32x4 __attribute__((ext_vector_type(4)));
typedef short bf16x8 __attribute__((ext_vector_type(8)));
typedef __bf16 bf2 __attribute__((ext_vector_type(2)));
#define GAS __attribute__((address_space(1)))
#define LAS __attribute__((address_space(3)))

constexpr int S = 2048, D = 576, F = 4608;

constexpr size_t WS_BT = 0;
constexpr size_t BT_BYTES = (size_t)2 * S * D * 2;
constexpr size_t WS_P = WS_BT + BT_BYTES;
constexpr size_t WS_CAND = WS_P + BT_BYTES;
constexpr size_t WS_PART = WS_CAND + 4096;

__device__ __forceinline__ float bflo(unsigned u) { return __uint_as_float(u << 16); }
__device__ __forceinline__ float bfhi(unsigned u) { return __uint_as_float(u & 0xffff0000u); }
__device__ __forceinline__ unsigned pk2(float a, float b) { bf2 p = {(__bf16)a, (__bf16)b}; return __builtin_bit_cast(unsigned, p); }
__device__ __forceinline__ float rbf(float f) { return bflo(pk2(f, f) & 0xffffu); }
__device__ __forceinline__ f32x4 mfma16(v4u a, v4u b, f32x4 c) {
  return __builtin_amdgcn_mfma_f32_16x16x32_bf16(__builtin_bit_cast(bf16x8, a), __builtin_bit_cast(bf16x8, b), c, 0, 0, 0);
}
__device__ __forceinline__ float dot2(unsigned a, unsigned b, float c) {
  return __builtin_amdgcn_fdot2_f32_bf16(__builtin_bit_cast(bf2, a), __builtin_bit_cast(bf2, b), c, false);
}
#define WAITV(n) asm volatile("s_waitcnt vmcnt(" #n ")" ::: "memory")
#define WAITL0() asm volatile("s_waitcnt lgkmcnt(0)" ::: "memory")
#define BAR() __builtin_amdgcn_s_barrier()
#define SCHED() __builtin_amdgcn_sched_barrier(0)

constexpr int K1_STAGE = 208 * 128;
constexpr int K1_STAT = 6 * K1_STAGE;

template <int NT>
__device__ __forceinline__ void k1_body(LAS char* L, const GAS char* src0, const GAS char* src1, const GAS char* src2, const GAS char* src3,
                                        int w, int lane, int mh, int grp, int n0, GAS char* bt_out) {
  auto stage = [&](int q, int slot) {
    LAS char* base = L + slot * K1_STAGE;
    __builtin_amdgcn_global_load_lds((const GAS unsigned*)(src0 + q * 128), (LAS unsigned*)(base + (w * 24) * 128), 16, 0, 0);
    __builtin_amdgcn_global_load_lds((const GAS unsigned*)(src1 + q * 128), (LAS unsigned*)(base + (w * 24 + 8) * 128), 16, 0, 0);
    __builtin_amdgcn_global_load_lds((const GAS unsigned*)(src2 + q * 128), (LAS unsigned*)(base + (w * 24 + 16) * 128), 16, 0, 0);
    if (lane < 16) __builtin_amdgcn_global_load_lds((const GAS unsigned*)(src3 + q * 128), (LAS unsigned*)(base + (192 + 2 * w) * 128), 16, 0, 0);
  };
#pragma unroll
  for (int q = 0; q < 6; ++q) stage(q, q);
  f32x4 acc[2][NT];
#pragma unroll
  for (int a = 0; a < 2; ++a)
#pragma unroll
    for (int b = 0; b < NT; ++b) acc[a][b] = f32x4{0.f, 0.f, 0.f, 0.f};
  float sx[2] = {0.f, 0.f}, sq[2] = {0.f, 0.f}, sw[NT];
#pragma unroll
  for (int b = 0; b < NT; ++b) sw[b] = 0.f;
  const int r15 = lane & 15, g = lane >> 4;
  const int off0 = r15 * 128 + ((g ^ (r15 >> 1)) * 16), off1 = off0 ^ 64;
  const int xrow = mh * 32 * 128, wrow = (64 + n0 * 16) * 128;
#pragma unroll
  for (int q = 0; q < 9; ++q) {
    if (q == 0) WAITV(20);
    else if (q <= 4) WAITV(16);
    else if (q == 5) WAITV(12);
    else if (q == 6) WAITV(8);
    else if (q == 7) WAITV(4);
    else WAITV(0);
    BAR();
    if (q >= 1 && q + 5 <= 8) stage(q + 5, (q + 5) % 6);
    LAS char* base = L + (q % 6) * K1_STAGE;
    v4u xf[2][2], wf[2][NT];
#pragma unroll
    for (int kk = 0; kk < 2; ++kk) {
      const int o = kk ? off1 : off0;
#pragma unroll
      for (int a = 0; a < 2; ++a) xf[kk][a] = *(LAS v4u*)(base + xrow + a * 2048 + o);
#pragma unroll
      for (int b = 0; b < NT; ++b) wf[kk][b] = *(LAS v4u*)(base + wrow + b * 2048 + o);
    }
#pragma unroll
    for (int kk = 0; kk < 2; ++kk) {
#pragma unroll
      for (int a = 0; a < 2; ++a)
#pragma unroll
        for (int b = 0; b < NT; ++b) acc[a][b] = mfma16(xf[kk][a], wf[kk][b], acc[a][b]);
      if (((2 * q + kk) & 3) == grp) {
#pragma unroll
        for (int a = 0; a < 2; ++a)
#pragma unroll
          for (int i = 0; i < 4; ++i) { sq[a] = dot2(xf[kk][a][i], xf[kk][a][i], sq[a]); sx[a] = dot2(xf[kk][a][i], 0x3f803f80u, sx[a]); }
      }
      if (kk == mh) {
#pragma unroll
        for (int b = 0; b < NT; ++b)
#pragma unroll
          for (int i = 0; i < 4; ++i) sw[b] = dot2(wf[kk][b][i], 0x3f803f80u, sw[b]);
      }
    }
  }
  LAS float* st = (LAS float*)(L + K1_STAT);
#pragma unroll
  for (int a = 0; a < 2; ++a) {
    sx[a] += __shfl_xor(sx[a], 16); sx[a] += __shfl_xor(sx[a], 32);
    sq[a] += __shfl_xor(sq[a], 16); sq[a] += __shfl_xor(sq[a], 32);
    if (lane < 16) { st[(grp * 64 + mh * 32 + a * 16 + lane) * 2] = sx[a]; st[(grp * 64 + mh * 32 + a * 16 + lane) * 2 + 1] = sq[a]; }
  }
#pragma unroll
  for (int b = 0; b < NT; ++b) {
    sw[b] += __shfl_xor(sw[b], 16); sw[b] += __shfl_xor(sw[b], 32);
    if (lane < 16) st[512 + mh * 144 + (n0 + b) * 16 + lane] = sw[b];
  }
  WAITL0(); BAR();
  if (w == 0) {
    float s = 0.f, qq = 0.f;
#pragma unroll
    for (int i = 0; i < 4; ++i) { s += st[(i * 64 + lane) * 2]; qq += st[(i * 64 + lane) * 2 + 1]; }
    const float mean = s * (1.0f / D);
    const float var = fmaxf(qq * (1.0f / D) - mean * mean, 0.f);
    const float sd = rbf(sqrtf(rbf(rbf(var) + 1e-5f)));
    st[800 + lane] = rbf(mean);
    st[864 + lane] = 1.0f / sd;
  }
  WAITL0(); BAR();
  f32x4 mu[2], ri[2];
#pragma unroll
  for (int a = 0; a < 2; ++a) {
    mu[a] = *(LAS f32x4*)(st + 800 + mh * 32 + a * 16 + g * 4);
    ri[a] = *(LAS f32x4*)(st + 864 + mh * 32 + a * 16 + g * 4);
  }
  const int gp = ((g & 1) << 1) | (g >> 1);
#pragma unroll
  for (int b = 0; b < NT; ++b) {
    const float ws = st[512 + (n0 + b) * 16 + r15] + st[512 + 144 + (n0 + b) * 16 + r15];
    unsigned p[2][2];
#pragma unroll
    for (int a = 0; a < 2; ++a) {
      float v[4];
#pragma unroll
      for (int i = 0; i < 4; ++i) v[i] = (acc[a][b][i] - mu[a][i] * ws) * ri[a][i];
      p[a][0] = pk2(v[0], v[1]); p[a][1] = pk2(v[2], v[3]);
    }
    auto r0 = __builtin_amdgcn_permlane16_swap(p[0][0], p[1][0], false, false);
    auto r1 = __builtin_amdgcn_permlane16_swap(p[0][1], p[1][1], false, false);
    v4u o = {r0[0], r1[0], r0[1], r1[1]};
    *(GAS v4u*)(bt_out + (n0 + b) * 1024 + (gp * 16 + r15) * 16) = o;
  }
}

__global__ void __launch_bounds__(512, 2) k1(const u16* __restrict__ x, const u16* __restrict__ wdr, const u16* __restrict__ wdi, char* __restrict__ ws) {
  __shared__ __attribute__((aligned(16))) char smem[6 * K1_STAGE + 4096];
  LAS char* L = (LAS char*)smem;
  const int lane = threadIdx.x & 63, w = __builtin_amdgcn_readfirstlane(threadIdx.x >> 6);
  const int xcd = blockIdx.x & 7, j = blockIdx.x >> 3;
  const int h = xcd >> 2, ct = j & 3, st = (xcd & 3) * 8 + (j >> 2);
  const GAS char* xb = (const GAS char*)x + (size_t)st * 64 * 1152;
  const GAS char* wb = (const GAS char*)(h ? wdi : wdr) + (size_t)ct * 144 * 1152;
  const GAS char* src[4];
#pragma unroll
  for (int m = 0; m < 4; ++m) {
    const int row = (m < 3 ? w * 24 + m * 8 : 192 + 2 * w) + (lane >> 3);
    const int rr = row < 208 ? row : 207;
    const int c = (lane & 7) ^ ((rr >> 1) & 7);
    src[m] = (rr < 64 ? xb + rr * 1152 : wb + (rr - 64) * 1152) + c * 16;
  }
  if (blockIdx.x == 0 && threadIdx.x < 256) *(GAS v4u*)((GAS char*)ws + WS_CAND + threadIdx.x * 16) = v4u{0u, 0u, 0u, 0u};
  const int mh = w & 1, grp = w >> 1;
  GAS char* bt_out = (GAS char*)ws + WS_BT + ((size_t)((h * 4 + ct) * 64 + st * 2 + mh) * 9) * 1024;
  if (grp == 0) k1_body<3>(L, src[0], src[1], src[2], src[3], w, lane, mh, grp, 0, bt_out);
  else k1_body<2>(L, src[0], src[1], src[2], src[3], w, lane, mh, grp, 1 + 2 * grp, bt_out);
}

constexpr int K2_P = 4;
template <int NT>
__device__ __forceinline__ void k2_body(LAS char* L, __amdgpu_buffer_rsrc_t ra, __amdgpu_buffer_rsrc_t rb, int a_voff, int a_soff0, int b_voff, int b_soff0,
                                        int lane, int kh, int grp, int n0, GAS char* p_out) {
  v4u as[K2_P][2], bs[K2_P][2][NT];
  auto loadA = [&](int i, int slot) {
    const int so = i < 16 ? a_soff0 + i * 128 : 0x40000000;
    as[slot][0] = __builtin_amdgcn_raw_buffer_load_b128(ra, a_voff, so, 0);
    as[slot][1] = __builtin_amdgcn_raw_buffer_load_b128(ra, a_voff + 32 * 4096, so, 0);
  };
  auto loadB = [&](int i, int slot) {
    const int so = i < 16 ? b_soff0 + i * 18432 : 0x40000000;
#pragma unroll
    for (int kk = 0; kk < 2; ++kk)
#pragma unroll
      for (int b = 0; b < NT; ++b) bs[slot][kk][b] = __builtin_amdgcn_raw_buffer_load_b128(rb, b_voff + b * 1024, so + kk * 9216, 0);
  };
  const int jj = grp * 64 + lane;
  const int wrow = jj >> 3;
  const int woff = kh * 8192 + wrow * 128 + (((jj & 7) ^ ((wrow >> 1) & 7)) * 16);
  const int r15 = lane & 15, g = lane >> 4;
  const int off0 = kh * 8192 + r15 * 128 + ((g ^ (r15 >> 1)) * 16), off1 = off0 ^ 64;
#pragma unroll
  for (int i = 0; i < K2_P; ++i) { loadA(i, i); loadB(i, i); }
  SCHED();
  *(LAS v4u*)(L + woff) = as[0][0];
  *(LAS v4u*)(L + woff + 4096) = as[0][1];
  loadA(K2_P, 0);
  f32x4 acc[4][NT];
#pragma unroll
  for (int a = 0; a < 4; ++a)
#pragma unroll
    for (int b = 0; b < NT; ++b) acc[a][b] = f32x4{0.f, 0.f, 0.f, 0.f};
  for (int it = 0; it < 16 / K2_P; ++it) {
#pragma unroll
    for (int u = 0; u < K2_P; ++u) {
      const int i = it * K2_P + u;
      __syncthreads();
      LAS char* rbase = L + (u & 1) * 16384;
      LAS char* wbase = L + ((u + 1) & 1) * 16384;
      v4u af[2][4];
#pragma unroll
      for (int kk = 0; kk < 2; ++kk)
#pragma unroll
        for (int a = 0; a < 4; ++a) af[kk][a] = *(LAS v4u*)(rbase + a * 2048 + (kk ? off1 : off0));
      const int sl = (u + 1) % K2_P;
      *(LAS v4u*)(wbase + woff) = as[sl][0];
      *(LAS v4u*)(wbase + woff + 4096) = as[sl][1];
      loadA(i + 1 + K2_P, sl);
      SCHED();
#pragma unroll
      for (int kk = 0; kk < 2; ++kk)
#pragma unroll
        for (int a = 0; a < 4; ++a)
#pragma unroll
          for (int b = 0; b < NT; ++b) acc[a][b] = mfma16(bs[u][kk][b], af[kk][a], acc[a][b]);
      SCHED();
      loadB(i + K2_P, u);
      SCHED();
    }
  }
  LAS char* red = L + 32768;
#pragma unroll
  for (int a = 0; a < 2; ++a)
#pragma unroll
    for (int b = 0; b < NT; ++b) {
      const f32x4 v = kh ? acc[a][b] : acc[2 + a][b];
      *(LAS f32x4*)(red + (((kh ^ 1) * 2 + a) * 9 + n0 + b) * 1024 + lane * 16) = v;
    }
  __syncthreads();
#pragma unroll
  for (int a = 0; a < 2; ++a)
#pragma unroll
    for (int b = 0; b < NT; ++b) {
      f32x4 v = kh ? acc[2 + a][b] : acc[a][b];
      v += *(LAS f32x4*)(red + ((kh * 2 + a) * 9 + n0 + b) * 1024 + lane * 16);
      v2u o = {pk2(v[0], v[1]), pk2(v[2], v[3])};
      *(GAS v2u*)(p_out + (size_t)(((kh * 2 + a) * 2 + (r15 >> 3)) * 36 + n0 + b) * 256 + (r15 & 7) * 32 + g * 8) = o;
    }
}

__global__ void __launch_bounds__(512, 2) k2(const u16* __restrict__ wsr, const u16* __restrict__ wsi, char* __restrict__ ws) {
  __shared__ __attribute__((aligned(16))) char smem[32768 + 36864];
  LAS char* L = (LAS char*)smem;
  const int lane = threadIdx.x & 63, w = __builtin_amdgcn_readfirstlane(threadIdx.x >> 6);
  const int xcd = blockIdx.x & 7, j = blockIdx.x >> 3;
  const int h = xcd >> 2, ct = j & 3, rt = (xcd & 3) * 8 + (j >> 2);
  const int kh = w & 1, grp = w >> 1;
  const __amdgpu_buffer_rsrc_t ra = __builtin_amdgcn_make_buffer_rsrc((void*)(h ? wsi : wsr), 0, S * S * 2, 0x00020000);
  const __amdgpu_buffer_rsrc_t rb = __builtin_amdgcn_make_buffer_rsrc((void*)(ws + WS_BT), 0, (int)BT_BYTES, 0x00020000);
  const int jj = grp * 64 + lane;
  const int a_voff = (jj >> 3) * 4096 + (jj & 7) * 16;
  const int a_soff0 = rt * 64 * 4096 + kh * 2048;
  const int n0 = grp == 0 ? 0 : 1 + 2 * grp;
  const int b_voff = lane * 16;
  const int b_soff0 = ((h * 4 + ct) * 64 + kh * 32) * 9216 + n0 * 1024;
  GAS char* p_out = (GAS char*)ws + WS_P + ((size_t)(h * 256 + rt * 8) * 36 + ct * 9) * 256;
  if (grp == 0) k2_body<3>(L, ra, rb, a_voff, a_soff0, b_voff, b_soff0, lane, kh, grp, n0, p_out);
  else k2_body<2>(L, ra, rb, a_voff, a_soff0, b_voff, b_soff0, lane, kh, grp, n0, p_out);
}

__device__ __forceinline__ float rowred(float v) {
  v += __shfl_xor(v, 1); v += __shfl_xor(v, 16); v += __shfl_xor(v, 32);
  return v;
}
__device__ __forceinline__ unsigned rkey(float r, int row) {
  unsigned u = pk2(r, r) & 0xffffu;
  unsigned k = (u & 0x8000u) ? (~u & 0xffffu) : (u | 0x8000u);
  if ((u & 0x7fffu) > 0x7f80u) k = 0;
  return (k << 16) | (unsigned)(0xffff - row);
}

__global__ void __launch_bounds__(576) k3(const u16* __restrict__ x, const u16* __restrict__ wr, u16* __restrict__ out, char* __restrict__ ws) {
  __shared__ float red[3][9][8];
  const int t = threadIdx.x, lane = t & 63, w = t >> 6;
  const int xcd = blockIdx.x & 7, j = blockIdx.x >> 3;
  const int t8 = (xcd & 3) * 64 + (xcd >> 2) * 32 + j;
  const int row = (t >> 1) & 7, ech = (t >> 4) * 2 + (t & 1);
  const v4u p1 = *(const GAS v4u*)((const GAS char*)ws + WS_P + (size_t)t8 * 9216 + t * 16);
  const v4u p2 = *(const GAS v4u*)((const GAS char*)ws + WS_P + (size_t)(256 + t8) * 9216 + t * 16);
  const size_t xo = (size_t)(t8 * 8 + row) * 1152 + ech * 16;
  const v4u xv = *(const GAS v4u*)((const GAS char*)x + xo);
  const v4u wv = *(const GAS v4u*)((const GAS char*)wr + ech * 16);
  float hv[8];
  float s = 0.f;
#pragma unroll
  for (int i = 0; i < 4; ++i) {
    const unsigned y = pk2(bflo(p1[i]) - bflo(p2[i]), bfhi(p1[i]) - bfhi(p2[i]));
    const unsigned hh = pk2(bflo(xv[i]) + bflo(y), bfhi(xv[i]) + bfhi(y));
    hv[2 * i] = bflo(hh); hv[2 * i + 1] = bfhi(hh);
    s += hv[2 * i] + hv[2 * i + 1];
  }
  s = rowred(s);
  if (lane < 16 && !(lane & 1)) red[0][w][row] = s;
  __syncthreads();
  float mean = 0.f;
#pragma unroll
  for (int i = 0; i < 9; ++i) mean += red[0][i][row];
  mean *= (1.0f / D);
  float q = 0.f;
#pragma unroll
  for (int i = 0; i < 8; ++i) { const float d = hv[i] - mean; q += d * d; }
  q = rowred(q);
  if (lane < 16 && !(lane & 1)) red[1][w][row] = q;
  __syncthreads();
  float var = 0.f;
#pragma unroll
  for (int i = 0; i < 9; ++i) var += red[1][i][row];
  var *= (1.0f / D);
  const float mb = rbf(mean), sd = rbf(sqrtf(rbf(rbf(var) + 1e-5f)));
  const float ri = 1.0f / sd;
  float r = 0.f;
  v4u o;
#pragma unroll
  for (int i = 0; i < 4; ++i) {
    const unsigned c = pk2(hv[2 * i] - mb, hv[2 * i + 1] - mb);
    const unsigned hn = pk2(bflo(c) * ri, bfhi(c) * ri);
    r = dot2(hn, wv[i], r);
    o[i] = pk2(hv[2 * i] + bflo(hn), hv[2 * i + 1] + bfhi(hn));
  }
  *(GAS v4u*)((GAS char*)out + xo) = o;
  r = rowred(r);
  if (lane < 16 && !(lane & 1)) red[2][w][row] = r;
  __syncthreads();
  if (w == 0) {
    float rr = 0.f;
#pragma unroll
    for (int i = 0; i < 9; ++i) rr += red[2][i][row];
    unsigned key = rkey(rr, t8 * 8 + row);
    unsigned st = (pk2(mb, mb) & 0xffffu) | (pk2(sd, sd) << 16);
#pragma unroll
    for (int m = 2; m <= 8; m <<= 1) {
      const unsigned k2 = __shfl_xor(key, m), s2 = __shfl_xor(st, m);
      if (k2 > key) { key = k2; st = s2; }
    }
    if (lane == 0) *(GAS v4u*)((GAS char*)ws + WS_CAND + blockIdx.x * 16) = v4u{key, 1u, st, 1u};
  }
}

__global__ void __launch_bounds__(576) k4(const u16* __restrict__ x, const u16* __restrict__ wg, const u16* __restrict__ wu, const u16* __restrict__ wd,
                                          char* __restrict__ ws) {
  __shared__ __attribute__((aligned(16))) float xs[D];
  __shared__ __attribute__((aligned(16))) float red[9][64];
  __shared__ __attribute__((aligned(16))) float act[32];
  __shared__ __attribute__((aligned(16))) float part[8][D];
  __shared__ unsigned best[4][2];
  const int t = threadIdx.x, lane = t & 63, w = t >> 6;
  const int xcd = blockIdx.x & 7, j = blockIdx.x >> 3;
  const int fs = xcd * 18 + j;
  v4u gw[4], uw[4], dw[4];
#pragma unroll
  for (int m = 0; m < 4; ++m) {
    const size_t o = (size_t)((t >> 2) + 144 * m) * (F * 2) + fs * 64 + (t & 3) * 16;
    gw[m] = *(const GAS v4u*)((const GAS char*)wg + o);
    uw[m] = *(const GAS v4u*)((const GAS char*)wu + o);
  }
  const int cc = t % 72, rg = t / 72;
#pragma unroll
  for (int m = 0; m < 4; ++m) dw[m] = *(const GAS v4u*)((const GAS char*)wd + (size_t)(fs * 32 + rg * 4 + m) * 1152 + cc * 16);
  if (t < 256) {
    const v4u c = *(const GAS v4u*)((const GAS char*)ws + WS_CAND + t * 16);
    unsigned key = c[0], st = c[2];
#pragma unroll
    for (int m = 1; m <= 32; m <<= 1) {
      const unsigned k2 = __shfl_xor(key, m), s2 = __shfl_xor(st, m);
      if (k2 > key) { key = k2; st = s2; }
    }
    if (lane == 0) { best[w][0] = key; best[w][1] = st; }
  }
  __syncthreads();
  unsigned key = best[0][0], st = best[0][1];
#pragma unroll
  for (int i = 1; i < 4; ++i) if (best[i][0] > key) { key = best[i][0]; st = best[i][1]; }
  const int idx = 0xffff - (key & 0xffffu);
  {
    const size_t po = ((size_t)(idx >> 3) * 36 + (t >> 4)) * 128 + (idx & 7) * 16 + (t & 15);
    const float p1 = bflo(((const GAS u16*)(ws + WS_P))[po]), p2 = bflo(((const GAS u16*)(ws + WS_P))[po + (size_t)256 * 36 * 128]);
    const float xv = bflo(x[idx * D + t]);
    const float hh = rbf(xv + rbf(p1 - p2));
    const float mb = bflo(st & 0xffffu), ri = 1.0f / bfhi(st);
    xs[t] = rbf(rbf(hh - mb) * ri);
  }
  __syncthreads();
  float v[16];
#pragma unroll
  for (int i = 0; i < 16; ++i) v[i] = 0.f;
#pragma unroll
  for (int m = 0; m < 4; ++m) {
    const float xv = xs[(t >> 2) + 144 * m];
#pragma unroll
    for (int i = 0; i < 4; ++i) {
      v[2 * i] += xv * bflo(gw[m][i]); v[2 * i + 1] += xv * bfhi(gw[m][i]);
      v[8 + 2 * i] += xv * bflo(uw[m][i]); v[8 + 2 * i + 1] += xv * bfhi(uw[m][i]);
    }
  }
#pragma unroll
  for (int i = 0; i < 16; ++i) { v[i] += __shfl_xor(v[i], 4); v[i] += __shfl_xor(v[i], 8); v[i] += __shfl_xor(v[i], 16); v[i] += __shfl_xor(v[i], 32); }
  if (lane < 4) {
#pragma unroll
    for (int i = 0; i < 8; ++i) { red[w][lane * 8 + i] = v[i]; red[w][32 + lane * 8 + i] = v[8 + i]; }
  }
  __syncthreads();
  if (t < 32) {
    float gg = 0.f, uu = 0.f;
#pragma unroll
    for (int i = 0; i < 9; ++i) { gg += red[i][t]; uu += red[i][32 + t]; }
    gg = rbf(gg); uu = rbf(uu);
    const float sg = rbf(gg / (1.0f + __expf(-gg)));
    act[t] = rbf(sg * uu);
  }
  __syncthreads();
  float y[8];
#pragma unroll
  for (int i = 0; i < 8; ++i) y[i] = 0.f;
#pragma unroll
  for (int m = 0; m < 4; ++m) {
    const float a = act[rg * 4 + m];
#pragma unroll
    for (int i = 0; i < 4; ++i) { y[2 * i] += a * bflo(dw[m][i]); y[2 * i + 1] += a * bfhi(dw[m][i]); }
  }
  *(f32x4*)&part[rg][cc * 8] = f32x4{y[0], y[1], y[2], y[3]};
  *(f32x4*)&part[rg][cc * 8 + 4] = f32x4{y[4], y[5], y[6], y[7]};
  __syncthreads();
  float ps = 0.f;
#pragma unroll
  for (int i = 0; i < 8; ++i) ps += part[i][t];
  ((GAS float*)(ws + WS_PART))[fs * D + t] = ps;
}

__global__ void __launch_bounds__(576) k5(const u16* __restrict__ x, u16* __restrict__ out, char* __restrict__ ws) {
  __shared__ float red[9][4];
  __shared__ unsigned best[4][2];
  const int t = threadIdx.x, lane = t & 63, w = t >> 6;
  const int rb = blockIdx.x;
  float v = ((const GAS float*)(ws + WS_PART))[(t >> 2) * D + rb * 4 + (t & 3)];
  if (t < 256) {
    const v4u c = *(const GAS v4u*)((const GAS char*)ws + WS_CAND + t * 16);
    unsigned key = c[0], st = c[2];
#pragma unroll
    for (int m = 1; m <= 32; m <<= 1) {
      const unsigned k2 = __shfl_xor(key, m), s2 = __shfl_xor(st, m);
      if (k2 > key) { key = k2; st = s2; }
    }
    if (lane == 0) { best[w][0] = key; best[w][1] = st; }
  }
  v += __shfl_xor(v, 4); v += __shfl_xor(v, 8); v += __shfl_xor(v, 16); v += __shfl_xor(v, 32);
  if (lane < 4) red[w][lane] = v;
  __syncthreads();
  if (t < 4) {
    unsigned key = best[0][0], st = best[0][1];
#pragma unroll
    for (int i = 1; i < 4; ++i) if (best[i][0] > key) { key = best[i][0]; st = best[i][1]; }
    const int idx = 0xffff - (key & 0xffffu);
    const unsigned k16 = key >> 16;
    const unsigned rbits = (k16 & 0x8000u) ? (k16 & 0x7fffu) : (~k16 & 0xffffu);
    const float r = bflo(rbits);
    float y = 0.f;
#pragma unroll
    for (int i = 0; i < 9; ++i) y += red[i][t];
    const int e = rb * 4 + t;
    const size_t po = ((size_t)(idx >> 3) * 36 + (e >> 4)) * 128 + (idx & 7) * 16 + (e & 15);
    const float p1 = bflo(((const GAS u16*)(ws + WS_P))[po]), p2 = bflo(((const GAS u16*)(ws + WS_P))[po + (size_t)256 * 36 * 128]);
    const float hh = rbf(bflo(x[idx * D + e]) + rbf(p1 - p2));
    const float mb = bflo(st & 0xffffu), ri = 1.0f / bfhi(st);
    const float hn = rbf(rbf(hh - mb) * ri);
    const float sg = rbf(1.0f / (1.0f + __expf(-r)));
    const float yy = rbf(rbf(y) * sg);
    out[idx * D + e] = pk2(hh + rbf(hn + yy), 0.f) & 0xffffu;
  }
}

extern "C" void kernel_launch(void* const* d_in, const int* in_sizes, int n_in, void* d_out, int out_size, void* d_ws, size_t ws_size,
                              hipStream_t stream) {
  const u16* x = (const u16*)d_in[0];
  const u16* wsr = (const u16*)d_in[1];
  const u16* wsi = (const u16*)d_in[2];
  const u16* wdr = (const u16*)d_in[3];
  const u16* wdi = (const u16*)d_in[4];
  const u16* wr = (const u16*)d_in[6];
  const u16* wg = (const u16*)d_in[7];
  const u16* wu = (const u16*)d_in[8];
  const u16* wd = (const u16*)d_in[9];
  u16* out = (u16*)d_out;
  char* ws = (char*)d_ws;
  k1<<<256, 512, 0, stream>>>(x, wdr, wdi, ws);
  k2<<<256, 512, 0, stream>>>(wsr, wsi, ws);
  k3<<<256, 576, 0, stream>>>(x, wr, out, ws);
  k4<<<144, 576, 0, stream>>>(x, wg, wu, wd, ws);
  k5<<<144, 576, 0, stream>>>(x, out, ws);
}
```

```cpp
#include <hip/hip_runtime.h>
#include <stdint.h>

typedef unsigned short u16;
typedef unsigned v4u __attribute__((ext_vector_type(4)));
typedef unsigned v2u __attribute__((ext_vector_type(2)));
typedef float f32x4 __attribute__((ext_vector_type(4)));
typedef short bf16x8 __attribute__((ext_vector_type(8)));
typedef __bf16 bf2 __attribute__((ext_vector_type(2)));
#define GAS __attribute__((address_space(1)))
#define LAS __attribute__((address_space(3)))

#define TS_DECL()
#define TS(i)
#define TS_STORE(kid, wsp)
#define TS_UNROLL _Pragma("unroll 1")
#define TS_ARGS
#define TS_PASS

#define DUMP_NK 5
#define DUMP_NB {256, 256, 256, 144, 144}
#define DUMP_NW {11, 10, 9, 9, 9}
constexpr int S = 2048, D = 576, F = 4608;

constexpr size_t WS_BT = 0;
constexpr size_t BT_BYTES = (size_t)2 * S * D * 2;
constexpr size_t WS_P = WS_BT + BT_BYTES;
constexpr size_t P_PART = (size_t)S * D * 2;
constexpr size_t WS_CAND = WS_P + 4 * P_PART;
constexpr size_t WS_PART = WS_CAND + 4096;

__device__ __forceinline__ float bflo(unsigned u) { return __uint_as_float(u << 16); }
__device__ __forceinline__ float bfhi(unsigned u) { return __uint_as_float(u & 0xffff0000u); }
__device__ __forceinline__ unsigned pk2(float a, float b) { bf2 p = {(__bf16)a, (__bf16)b}; return __builtin_bit_cast(unsigned, p); }
__device__ __forceinline__ float rbf(float f) { return bflo(pk2(f, f) & 0xffffu); }
__device__ __forceinline__ f32x4 mfma16(v4u a, v4u b, f32x4 c) {
  return __builtin_amdgcn_mfma_f32_16x16x32_bf16(__builtin_bit_cast(bf16x8, a), __builtin_bit_cast(bf16x8, b), c, 0, 0, 0);
}
__device__ __forceinline__ float dot2(unsigned a, unsigned b, float c) {
  return __builtin_amdgcn_fdot2_f32_bf16(__builtin_bit_cast(bf2, a), __builtin_bit_cast(bf2, b), c, false);
}
#define WAITV(n) asm volatile("s_waitcnt vmcnt(" #n ")" ::: "memory")
#define WAITL0() __builtin_amdgcn_s_waitcnt(0xC07F)
#define BAR() do { asm volatile("" ::: "memory"); __builtin_amdgcn_s_barrier(); asm volatile("" ::: "memory"); } while (0)
#define SCHED() __builtin_amdgcn_sched_barrier(0)

#define K1_AUXX 0
#define K1_AUXW 0
#define K1_ROT 1
#define K1_PF 1
#define GLDS(rsrc, lds, voff, soff, aux) __builtin_amdgcn_raw_ptr_buffer_load_lds(rsrc, (LAS void*)(lds), 16, voff, soff, 0, aux)
__device__ __forceinline__ float dpp_add(float v, const int ctrl_sel) {
  if (ctrl_sel == 0) return v + __builtin_bit_cast(float, __builtin_amdgcn_update_dpp(0, __builtin_bit_cast(int, v), 0xB1, 0xf, 0xf, true));
  if (ctrl_sel == 1) return v + __builtin_bit_cast(float, __builtin_amdgcn_update_dpp(0, __builtin_bit_cast(int, v), 0x4E, 0xf, 0xf, true));
  return v + __builtin_bit_cast(float, __builtin_amdgcn_update_dpp(0, __builtin_bit_cast(int, v), 0x141, 0xf, 0xf, true));
}
__device__ __forceinline__ float red8(float v) { return dpp_add(dpp_add(dpp_add(v, 0), 1), 2); }

constexpr int K1_XN = 0;
constexpr int K1_RING = 9 * 8192;
constexpr int K1_WST = 18432;

template <int NT>
__device__ __forceinline__ void k1_mma(LAS char* L, int lane, int mh, int n0, int rot, GAS char* bt_out, char* ws TS_ARGS) {
  const int r15 = lane & 15, g = lane >> 4;
  const int off0 = r15 * 128 + ((g ^ (r15 >> 1)) * 16), off1 = off0 ^ 64;
  f32x4 acc[2][NT];
#pragma unroll
  for (int a = 0; a < 2; ++a)
#pragma unroll
    for (int b = 0; b < NT; ++b) acc[a][b] = f32x4{0.f, 0.f, 0.f, 0.f};
  v4u xf[2][2], wf[2][NT];
  auto rd = [&](int buf, int i, int o) {
    int q = i + rot; q = q >= 9 ? q - 9 : q;
    LAS char* xb = L + K1_XN + q * 8192 + mh * 4096 + o;
    LAS char* wb = L + K1_RING + (i & 3) * K1_WST + n0 * 2048 + o;
#pragma unroll
    for (int a = 0; a < 2; ++a) xf[buf][a] = *(LAS v4u*)(xb + a * 2048);
#pragma unroll
    for (int b = 0; b < NT; ++b) wf[buf][b] = *(LAS v4u*)(wb + b * 2048);
  };
  auto mm = [&](int buf) {
#pragma unroll
    for (int b = 0; b < NT; ++b)
#pragma unroll
      for (int a = 0; a < 2; ++a) acc[a][b] = mfma16(xf[buf][a], wf[buf][b], acc[a][b]);
  };
  rd(0, 0, off0);
  TS_UNROLL
  for (int i = 0; i < 9; ++i) {
    rd(1, i, off1);
    SCHED();
    mm(0);
    SCHED();
    WAITL0();
    BAR();
    TS(3 + i);
    rd(0, i < 8 ? i + 1 : 0, off0);
    SCHED();
    mm(1);
    SCHED();
  }
  TS(12);
  const int gp = ((g & 1) << 1) | (g >> 1);
#pragma unroll
  for (int b = 0; b < NT; ++b) {
    auto r0 = __builtin_amdgcn_permlane16_swap(pk2(acc[0][b][0], acc[0][b][1]), pk2(acc[1][b][0], acc[1][b][1]), false, false);
    auto r1 = __builtin_amdgcn_permlane16_swap(pk2(acc[0][b][2], acc[0][b][3]), pk2(acc[1][b][2], acc[1][b][3]), false, false);
    v4u o = {r0[0], r1[0], r0[1], r1[1]};
    *(GAS v4u*)(bt_out + (n0 + b) * 1024 + (gp * 16 + r15) * 16) = o;
  }
  TS(13);
}

__global__ void __launch_bounds__(704, 3) k1(const u16* __restrict__ x, const u16* __restrict__ wdr, const u16* __restrict__ wdi, const u16* __restrict__ wsr,
                                             const u16* __restrict__ wsi, char* __restrict__ ws) {
  __shared__ __attribute__((aligned(16))) char smem[9 * 8192 + 4 * K1_WST];
  TS_DECL();
  LAS char* L = (LAS char*)smem;
  const int lane = threadIdx.x & 63, w = __builtin_amdgcn_readfirstlane(threadIdx.x >> 6);
  const int xcd = blockIdx.x & 7, j = blockIdx.x >> 3;
  const int h = xcd >> 2, ct = j & 3, st = (xcd & 3) * 8 + (j >> 2);
  const int rotw = K1_ROT ? (j >> 2) : 0;
  if (w >= 8) {
    if (w < 10) {
      const int l = w - 8;
      const __amdgpu_buffer_rsrc_t rw = __builtin_amdgcn_make_buffer_rsrc((void*)(h ? wdi : wdr), 0, D * D * 2, 0x00020000);
      const int rl = lane >> 3;
      const int v_even = rl * 1152 + (((lane & 7) ^ ((rl >> 1) & 7)) * 16);
      const int v_odd = rl * 1152 + (((lane & 7) ^ (((rl + 8) >> 1) & 7)) * 16);
      const int sbase = (ct * 144 + l * 72) * 1152;
      auto stage = [&](int i) {
        int q = i + rotw; q = q >= 9 ? q - 9 : q;
        LAS char* dst = L + K1_RING + (i & 3) * K1_WST + l * 9216;
#pragma unroll
        for (int p = 0; p < 9; ++p) GLDS(rw, dst + p * 1024, ((p + l) & 1) ? v_odd : v_even, sbase + p * 8 * 1152 + q * 128, K1_AUXW);
      };
      stage(0); stage(1); stage(2); stage(3);
      WAITV(27);
      BAR();
#pragma unroll 1
      for (int i = 0; i < 9; ++i) {
        if (i <= 5) WAITV(18);
        else if (i == 6) WAITV(9);
        else WAITV(0);
        BAR();
        if (i + 4 < 9) stage(i + 4);
      }
    } else {
      if (blockIdx.x == 0) {
#pragma unroll
        for (int i = 0; i < 4; ++i) *(GAS v4u*)((GAS char*)ws + WS_CAND + (i * 64 + lane) * 16) = v4u{0u, 0u, 0u, 0u};
      }
      if (K1_PF) {
        const __amdgpu_buffer_rsrc_t ra = __builtin_amdgcn_make_buffer_rsrc((void*)(h ? wsi : wsr), 0, S * S * 2, 0x00020000);
        const int voff = (lane >> 1) * 4096 + (lane & 1) * 128;
        const int sb = ((xcd & 1) * 1024 + j * 32) * 4096 + ((xcd >> 1) & 1) * 2048;
        unsigned acc = 0;
#pragma unroll
        for (int m = 0; m < 8; m += K1_PF) {
          unsigned v[K1_PF];
#pragma unroll
          for (int n = 0; n < K1_PF; ++n) v[n] = __builtin_amdgcn_raw_buffer_load_b32(ra, voff, sb + (m + n) * 256, 2);
#pragma unroll
          for (int n = 0; n < K1_PF; ++n) acc ^= v[n];
          asm volatile("" : "+v"(acc));
          SCHED();
        }
      }
#pragma unroll 1
      for (int i = 0; i < 10; ++i) BAR();
    }
  } else {
    const __amdgpu_buffer_rsrc_t rx = __builtin_amdgcn_make_buffer_rsrc((void*)x, 0, S * D * 2, 0x00020000);
    const int rotx = K1_ROT ? ct * 2 : 0;
    const int row = w * 8 + (lane >> 3);
    v4u xv[9];
#pragma unroll
    for (int i = 0; i < 9; ++i) {
      int q = i + rotx; q = q >= 9 ? q - 9 : q;
      xv[i] = __builtin_amdgcn_raw_buffer_load_b128(rx, (lane >> 3) * 1152 + (lane & 7) * 16, (st * 64 + w * 8) * 1152 + q * 128, K1_AUXX);
    }
    SCHED();
    TS(0);
    float s = 0.f;
#pragma unroll
    for (int i = 0; i < 9; ++i)
#pragma unroll
      for (int c = 0; c < 4; ++c) s = dot2(xv[i][c], 0x3f803f80u, s);
    const float mean = red8(s) * (1.0f / D);
    float q0 = 0.f, q1 = 0.f;
#pragma unroll
    for (int i = 0; i < 9; ++i)
#pragma unroll
      for (int c = 0; c < 4; ++c) {
        const float d0 = bflo(xv[i][c]) - mean, d1 = bfhi(xv[i][c]) - mean;
        q0 += d0 * d0; q1 += d1 * d1;
      }
    const float var = red8(q0 + q1) * (1.0f / D);
    const float mb = rbf(mean), ri = 1.0f / rbf(sqrtf(rbf(rbf(var) + 1e-5f)));
    TS(1);
    const int wo = row * 128 + (((lane & 7) ^ ((row >> 1) & 7)) * 16);
#pragma unroll
    for (int i = 0; i < 9; ++i) {
      int q = i + rotx; q = q >= 9 ? q - 9 : q;
      v4u o;
#pragma unroll
      for (int c = 0; c < 4; ++c) {
        const unsigned d = pk2(bflo(xv[i][c]) - mb, bfhi(xv[i][c]) - mb);
        o[c] = pk2(bflo(d) * ri, bfhi(d) * ri);
      }
      *(LAS v4u*)(L + K1_XN + q * 8192 + wo) = o;
    }
    WAITL0();
    BAR();
    TS(2);
    const int mh = w & 1, grp = w >> 1;
    GAS char* bt_out = (GAS char*)ws + WS_BT + ((size_t)((h * 4 + ct) * 64 + st * 2 + mh) * 9) * 1024;
    if (grp == 0) k1_mma<3>(L, lane, mh, 0, rotw, bt_out, ws TS_PASS);
    else k1_mma<2>(L, lane, mh, 1 + 2 * grp, rotw, bt_out, ws TS_PASS);
  }
  TS_STORE(0, ws);
}

#define K2_AUXA 0
#define K2_LA 3
#define K2_AUXB 0
constexpr int K2_ST = 16384 + 18432;

template <int NT>
__device__ __forceinline__ void k2_mma(LAS char* L, int lane, int m, int n0, GAS char* p_out, char* ws TS_ARGS) {
  const int r15 = lane & 15, g = lane >> 4;
  const int off0 = m * 4096 + r15 * 128 + ((g ^ (r15 >> 1)) * 16), off1 = off0 ^ 64;
  const int boff = 16384 + n0 * 1024 + lane * 16;
  f32x4 acc[2][NT];
#pragma unroll
  for (int a = 0; a < 2; ++a)
#pragma unroll
    for (int b = 0; b < NT; ++b) acc[a][b] = f32x4{0.f, 0.f, 0.f, 0.f};
  v4u af[2][2], bf[2][NT];
  auto rd = [&](int buf, int i) {
    LAS char* sb = L + (i & 3) * K2_ST;
#pragma unroll
    for (int a = 0; a < 2; ++a) af[buf][a] = *(LAS v4u*)(sb + (buf ? off1 : off0) + a * 2048);
#pragma unroll
    for (int b = 0; b < NT; ++b) bf[buf][b] = *(LAS v4u*)(sb + boff + buf * 9216 + b * 1024);
  };
  auto mm = [&](int buf) {
#pragma unroll
    for (int b = 0; b < NT; ++b)
#pragma unroll
      for (int a = 0; a < 2; ++a) acc[a][b] = mfma16(bf[buf][b], af[buf][a], acc[a][b]);
  };
  BAR();
  TS(0);
  rd(0, 0);
  TS_UNROLL
  for (int i = 0; i < 16; ++i) {
    rd(1, i);
    SCHED();
    mm(0);
    SCHED();
    WAITL0();
    BAR();
    TS(1 + i);
    rd(0, i + 1);
    SCHED();
    mm(1);
    SCHED();
  }
  TS(17);
#pragma unroll
  for (int b = 0; b < NT; ++b) {
    auto r0 = __builtin_amdgcn_permlane16_swap(pk2(acc[0][b][0], acc[0][b][1]), pk2(acc[1][b][0], acc[1][b][1]), false, false);
    auto r1 = __builtin_amdgcn_permlane16_swap(pk2(acc[0][b][2], acc[0][b][3]), pk2(acc[1][b][2], acc[1][b][3]), false, false);
    v4u o = {r0[0], r1[0], r0[1], r1[1]};
    *(GAS v4u*)(p_out + (size_t)(((m * 2 + (g & 1)) * 2 + (r15 >> 3)) * 36 + n0 + b) * 256 + (r15 & 7) * 32 + (g >> 1) * 16) = o;
  }
  TS(18);
}

__global__ void __launch_bounds__(640, 3) k2(const u16* __restrict__ wsr, const u16* __restrict__ wsi, char* __restrict__ ws) {
  __shared__ __attribute__((aligned(16))) char smem[4 * K2_ST];
  TS_DECL();
  LAS char* L = (LAS char*)smem;
  const int lane = threadIdx.x & 63, w = __builtin_amdgcn_readfirstlane(threadIdx.x >> 6);
  const int xcd = blockIdx.x & 7, j = blockIdx.x >> 3;
  const int kq = xcd >> 1, h = kq >> 1, khalf = kq & 1, ct = j & 3, rt = (xcd & 1) * 8 + (j >> 2);
  if (w < 2) {
    const int l = w;
    const __amdgpu_buffer_rsrc_t ra = __builtin_amdgcn_make_buffer_rsrc((void*)(h ? wsi : wsr), 0, S * S * 2, 0x00020000);
    const __amdgpu_buffer_rsrc_t rb = __builtin_amdgcn_make_buffer_rsrc((void*)(ws + WS_BT), 0, (int)BT_BYTES, 0x00020000);
    const int rl = lane >> 3;
    const int v_even = rl * 4096 + (((lane & 7) ^ ((rl >> 1) & 7)) * 16);
    const int v_odd = rl * 4096 + (((lane & 7) ^ (((rl + 8) >> 1) & 7)) * 16);
    const int sa = (rt * 128 + l * 64) * 4096 + khalf * 2048;
    const int sb = ((h * 4 + ct) * 64 + khalf * 32 + l) * 9216;
    auto stage = [&](int i) {
      LAS char* dst = L + (i & 3) * K2_ST;
#pragma unroll
      for (int p = 0; p < 9; ++p) {
        GLDS(rb, dst + 16384 + l * 9216 + p * 1024, lane * 16, sb + i * 18432 + p * 1024, K2_AUXB);
        if (p < 8) GLDS(ra, dst + l * 8192 + p * 1024, (p & 1) ? v_odd : v_even, sa + p * 8 * 4096 + i * 128, K2_AUXA);
      }
    };
    stage(0); stage(1);
    WAITV(17);
    BAR();
    stage(2);
#pragma unroll 1
    for (int i = 0; i < 16; ++i) {
      if (i <= 13) WAITV(17);
      else WAITV(0);
      BAR();
      if (i + 3 < 16) stage(i + 3);
    }
  } else {
    const int cw = w - 2, m = cw & 3, nh = cw >> 2;
    GAS char* p_out = (GAS char*)ws + WS_P + ((size_t)(kq * 256 + rt * 16) * 36 + ct * 9) * 256;
    if (nh == 0) k2_mma<5>(L, lane, m, 0, p_out, ws TS_PASS);
    else k2_mma<4>(L, lane, m, 5, p_out, ws TS_PASS);
  }
  TS_STORE(1, ws);
}

__device__ __forceinline__ float rowred(float v) {
  v += __shfl_xor(v, 1); v += __shfl_xor(v, 16); v += __shfl_xor(v, 32);
  return v;
}
__device__ __forceinline__ unsigned rkey(float r, int row) {
  unsigned u = pk2(r, r) & 0xffffu;
  unsigned k = (u & 0x8000u) ? (~u & 0xffffu) : (u | 0x8000u);
  if ((u & 0x7fffu) > 0x7f80u) k = 0;
  return (k << 16) | (unsigned)(0xffff - row);
}

__global__ void __launch_bounds__(576) k3(const u16* __restrict__ x, const u16* __restrict__ wr, u16* __restrict__ out, char* __restrict__ ws) {
  TS_DECL();
  __shared__ float red[3][9][8];
  const int t = threadIdx.x, lane = t & 63, w = t >> 6;
  const int xcd = blockIdx.x & 7, j = blockIdx.x >> 3;
  const int t8 = (xcd & 1) * 128 + (xcd >> 1) * 32 + j;
  const int row = (t >> 1) & 7, ech = (t >> 4) * 2 + (t & 1);
  v4u pp[4];
#pragma unroll
  for (int i = 0; i < 4; ++i) pp[i] = *(const GAS v4u*)((const GAS char*)ws + WS_P + i * P_PART + (size_t)t8 * 9216 + t * 16);
  const size_t xo = (size_t)(t8 * 8 + row) * 1152 + ech * 16;
  const v4u xv = *(const GAS v4u*)((const GAS char*)x + xo);
  const v4u wv = *(const GAS v4u*)((const GAS char*)wr + ech * 16);
  float hv[8];
  float s = 0.f;
#pragma unroll
  for (int i = 0; i < 4; ++i) {
    const unsigned p1 = pk2(bflo(pp[0][i]) + bflo(pp[1][i]), bfhi(pp[0][i]) + bfhi(pp[1][i]));
    const unsigned p2 = pk2(bflo(pp[2][i]) + bflo(pp[3][i]), bfhi(pp[2][i]) + bfhi(pp[3][i]));
    const unsigned y = pk2(bflo(p1) - bflo(p2), bfhi(p1) - bfhi(p2));
    const unsigned hh = pk2(bflo(xv[i]) + bflo(y), bfhi(xv[i]) + bfhi(y));
    hv[2 * i] = bflo(hh); hv[2 * i + 1] = bfhi(hh);
    s += hv[2 * i] + hv[2 * i + 1];
  }
  s = rowred(s);
  if (lane < 16 && !(lane & 1)) red[0][w][row] = s;
  __syncthreads();
  float mean = 0.f;
#pragma unroll
  for (int i = 0; i < 9; ++i) mean += red[0][i][row];
  mean *= (1.0f / D);
  float q = 0.f;
#pragma unroll
  for (int i = 0; i < 8; ++i) { const float d = hv[i] - mean; q += d * d; }
  q = rowred(q);
  if (lane < 16 && !(lane & 1)) red[1][w][row] = q;
  __syncthreads();
  float var = 0.f;
#pragma unroll
  for (int i = 0; i < 9; ++i) var += red[1][i][row];
  var *= (1.0f / D);
  const float mb = rbf(mean), sd = rbf(sqrtf(rbf(rbf(var) + 1e-5f)));
  const float ri = 1.0f / sd;
  float r = 0.f;
  v4u o;
#pragma unroll
  for (int i = 0; i < 4; ++i) {
    const unsigned c = pk2(hv[2 * i] - mb, hv[2 * i + 1] - mb);
    const unsigned hn = pk2(bflo(c) * ri, bfhi(c) * ri);
    r = dot2(hn, wv[i], r);
    o[i] = pk2(hv[2 * i] + bflo(hn), hv[2 * i + 1] + bfhi(hn));
  }
  *(GAS v4u*)((GAS char*)out + xo) = o;
  r = rowred(r);
  if (lane < 16 && !(lane & 1)) red[2][w][row] = r;
  __syncthreads();
  if (w == 0) {
    float rr = 0.f;
#pragma unroll
    for (int i = 0; i < 9; ++i) rr += red[2][i][row];
    unsigned key = rkey(rr, t8 * 8 + row);
    unsigned st = (pk2(mb, mb) & 0xffffu) | (pk2(sd, sd) << 16);
#pragma unroll
    for (int m = 2; m <= 8; m <<= 1) {
      const unsigned k2 = __shfl_xor(key, m), s2 = __shfl_xor(st, m);
      if (k2 > key) { key = k2; st = s2; }
    }
    if (lane == 0) *(GAS v4u*)((GAS char*)ws + WS_CAND + blockIdx.x * 16) = v4u{key, 1u, st, 1u};
  }
  TS_STORE(2, ws);
}

__global__ void __launch_bounds__(576) k4(const u16* __restrict__ x, const u16* __restrict__ wg, const u16* __restrict__ wu, const u16* __restrict__ wd,
                                          char* __restrict__ ws) {
  TS_DECL();
  __shared__ __attribute__((aligned(16))) float xs[D];
  __shared__ __attribute__((aligned(16))) float red[9][64];
  __shared__ __attribute__((aligned(16))) float act[32];
  __shared__ __attribute__((aligned(16))) float part[8][D];
  __shared__ unsigned best[4][2];
  const int t = threadIdx.x, lane = t & 63, w = t >> 6;
  const int xcd = blockIdx.x & 7, j = blockIdx.x >> 3;
  const int fs = xcd * 18 + j;
  v4u gw[4], uw[4], dw[4];
#pragma unroll
  for (int m = 0; m < 4; ++m) {
    const size_t o = (size_t)((t >> 2) + 144 * m) * (F * 2) + fs * 64 + (t & 3) * 16;
    gw[m] = *(const GAS v4u*)((const GAS char*)wg + o);
    uw[m] = *(const GAS v4u*)((const GAS char*)wu + o);
  }
  const int cc = t % 72, rg = t / 72;
#pragma unroll
  for (int m = 0; m < 4; ++m) dw[m] = *(const GAS v4u*)((const GAS char*)wd + (size_t)(fs * 32 + rg * 4 + m) * 1152 + cc * 16);
  if (t < 256) {
    const v4u c = *(const GAS v4u*)((const GAS char*)ws + WS_CAND + t * 16);
    unsigned key = c[0], st = c[2];
#pragma unroll
    for (int m = 1; m <= 32; m <<= 1) {
      const unsigned k2 = __shfl_xor(key, m), s2 = __shfl_xor(st, m);
      if (k2 > key) { key = k2; st = s2; }
    }
    if (lane == 0) { best[w][0] = key; best[w][1] = st; }
  }
  __syncthreads();
  unsigned key = best[0][0], st = best[0][1];
#pragma unroll
  for (int i = 1; i < 4; ++i) if (best[i][0] > key) { key = best[i][0]; st = best[i][1]; }
  const int idx = 0xffff - (key & 0xffffu);
  {
    const size_t po = ((size_t)(idx >> 3) * 36 + (t >> 4)) * 128 + (idx & 7) * 16 + (t & 15);
    const GAS u16* pq = (const GAS u16*)(ws + WS_P) + po;
    const float p1 = rbf(bflo(pq[0]) + bflo(pq[P_PART / 2])), p2 = rbf(bflo(pq[P_PART]) + bflo(pq[3 * P_PART / 2]));
    const float xv = bflo(x[idx * D + t]);
    const float hh = rbf(xv + rbf(p1 - p2));
    const float mb = bflo(st & 0xffffu), ri = 1.0f / bfhi(st);
    xs[t] = rbf(rbf(hh - mb) * ri);
  }
  __syncthreads();
  float v[16];
#pragma unroll
  for (int i = 0; i < 16; ++i) v[i] = 0.f;
#pragma unroll
  for (int m = 0; m < 4; ++m) {
    const float xv = xs[(t >> 2) + 144 * m];
#pragma unroll
    for (int i = 0; i < 4; ++i) {
      v[2 * i] += xv * bflo(gw[m][i]); v[2 * i + 1] += xv * bfhi(gw[m][i]);
      v[8 + 2 * i] += xv * bflo(uw[m][i]); v[8 + 2 * i + 1] += xv * bfhi(uw[m][i]);
    }
  }
#pragma unroll
  for (int i = 0; i < 16; ++i) { v[i] += __shfl_xor(v[i], 4); v[i] += __shfl_xor(v[i], 8); v[i] += __shfl_xor(v[i], 16); v[i] += __shfl_xor(v[i], 32); }
  if (lane < 4) {
#pragma unroll
    for (int i = 0; i < 8; ++i) { red[w][lane * 8 + i] = v[i]; red[w][32 + lane * 8 + i] = v[8 + i]; }
  }
  __syncthreads();
  if (t < 32) {
    float gg = 0.f, uu = 0.f;
#pragma unroll
    for (int i = 0; i < 9; ++i) { gg += red[i][t]; uu += red[i][32 + t]; }
    gg = rbf(gg); uu = rbf(uu);
    const float sg = rbf(gg / (1.0f + __expf(-gg)));
    act[t] = rbf(sg * uu);
  }
  __syncthreads();
  float y[8];
#pragma unroll
  for (int i = 0; i < 8; ++i) y[i] = 0.f;
#pragma unroll
  for (int m = 0; m < 4; ++m) {
    const float a = act[rg * 4 + m];
#pragma unroll
    for (int i = 0; i < 4; ++i) { y[2 * i] += a * bflo(dw[m][i]); y[2 * i + 1] += a * bfhi(dw[m][i]); }
  }
  *(f32x4*)&part[rg][cc * 8] = f32x4{y[0], y[1], y[2], y[3]};
  *(f32x4*)&part[rg][cc * 8 + 4] = f32x4{y[4], y[5], y[6], y[7]};
  __syncthreads();
  float ps = 0.f;
#pragma unroll
  for (int i = 0; i < 8; ++i) ps += part[i][t];
  ((GAS float*)(ws + WS_PART))[fs * D + t] = ps;
  TS_STORE(3, ws);
}

__global__ void __launch_bounds__(576) k5(const u16* __restrict__ x, u16* __restrict__ out, char* __restrict__ ws) {
  TS_DECL();
  __shared__ float red[9][4];
  __shared__ unsigned best[4][2];
  const int t = threadIdx.x, lane = t & 63, w = t >> 6;
  const int rb = blockIdx.x;
  float v = ((const GAS float*)(ws + WS_PART))[(t >> 2) * D + rb * 4 + (t & 3)];
  if (t < 256) {
    const v4u c = *(const GAS v4u*)((const GAS char*)ws + WS_CAND + t * 16);
    unsigned key = c[0], st = c[2];
#pragma unroll
    for (int m = 1; m <= 32; m <<= 1) {
      const unsigned k2 = __shfl_xor(key, m), s2 = __shfl_xor(st, m);
      if (k2 > key) { key = k2; st = s2; }
    }
    if (lane == 0) { best[w][0] = key; best[w][1] = st; }
  }
  v += __shfl_xor(v, 4); v += __shfl_xor(v, 8); v += __shfl_xor(v, 16); v += __shfl_xor(v, 32);
  if (lane < 4) red[w][lane] = v;
  __syncthreads();
  if (t < 4) {
    unsigned key = best[0][0], st = best[0][1];
#pragma unroll
    for (int i = 1; i < 4; ++i) if (best[i][0] > key) { key = best[i][0]; st = best[i][1]; }
    const int idx = 0xffff - (key & 0xffffu);
    const unsigned k16 = key >> 16;
    const unsigned rbits = (k16 & 0x8000u) ? (k16 & 0x7fffu) : (~k16 & 0xffffu);
    const float r = bflo(rbits);
    float y = 0.f;
#pragma unroll
    for (int i = 0; i < 9; ++i) y += red[i][t];
    const int e = rb * 4 + t;
    const size_t po = ((size_t)(idx >> 3) * 36 + (e >> 4)) * 128 + (idx & 7) * 16 + (e & 15);
    const GAS u16* pq = (const GAS u16*)(ws + WS_P) + po;
    const float p1 = rbf(bflo(pq[0]) + bflo(pq[P_PART / 2])), p2 = rbf(bflo(pq[P_PART]) + bflo(pq[3 * P_PART / 2]));
    const float hh = rbf(bflo(x[idx * D + e]) + rbf(p1 - p2));
    const float mb = bflo(st & 0xffffu), ri = 1.0f / bfhi(st);
    const float hn = rbf(rbf(hh - mb) * ri);
    const float sg = rbf(1.0f / (1.0f + __expf(-r)));
    const float yy = rbf(rbf(y) * sg);
    out[idx * D + e] = pk2(hh + rbf(hn + yy), 0.f) & 0xffffu;
  }
  TS_STORE(4, ws);
}

extern "C" void kernel_launch(void* const* d_in, const int* in_sizes, int n_in, void* d_out, int out_size, void* d_ws, size_t ws_size,
                              hipStream_t stream) {
  const u16* x = (const u16*)d_in[0];
  const u16* wsr = (const u16*)d_in[1];
  const u16* wsi = (const u16*)d_in[2];
  const u16* wdr = (const u16*)d_in[3];
  const u16* wdi = (const u16*)d_in[4];
  const u16* wr = (const u16*)d_in[6];
  const u16* wg = (const u16*)d_in[7];
  const u16* wu = (const u16*)d_in[8];
  const u16* wd = (const u16*)d_in[9];
  u16* out = (u16*)d_out;
  char* ws = (char*)d_ws;
  k1<<<256, 704, 0, stream>>>(x, wdr, wdi, wsr, wsi, ws);
  k2<<<256, 640, 0, stream>>>(wsr, wsi, ws);
  k3<<<256, 576, 0, stream>>>(x, wr, out, ws);
  k4<<<144, 576, 0, stream>>>(x, wg, wu, wd, ws);
  k5<<<144, 576, 0, stream>>>(x, out, ws);
}
```

```cpp
#define PF_F 1
#include <hip/hip_runtime.h>
#include <stdint.h>

typedef unsigned short u16;
typedef unsigned long long u64;
typedef unsigned v4u __attribute__((ext_vector_type(4)));
typedef unsigned v2u __attribute__((ext_vector_type(2)));
typedef float f32x4 __attribute__((ext_vector_type(4)));
typedef short bf16x8 __attribute__((ext_vector_type(8)));
typedef short s16x4 __attribute__((ext_vector_type(4)));
typedef __bf16 bf2 __attribute__((ext_vector_type(2)));
#define GAS __attribute__((address_space(1)))
#define LAS __attribute__((address_space(3)))
#define RLX_AGENT __ATOMIC_RELAXED, __HIP_MEMORY_SCOPE_AGENT

constexpr int S = 2048, D = 576, F = 4608;

constexpr int WS_EPOCH = 0;
constexpr int WS_F1 = 4096;
constexpr int WS_F2 = WS_F1 + 32768;
constexpr int WS_CAND = WS_F2 + 32768;
constexpr int WS_G = WS_CAND + 4096;
constexpr int WS_BT = 1 << 20;
constexpr int BT_BYTES = 2 * S * D * 2;
constexpr int WS_P = WS_BT + BT_BYTES;
constexpr int P_PART = S * D * 2;
constexpr int WS_END = WS_P + 4 * P_PART;

constexpr int P1_ST = 26624;
constexpr int L_STAT = 3 * P1_ST;
constexpr int L_MISC = 86016;
constexpr int P2_BST = 18432;
constexpr int P2_A = 98304;
constexpr int L_GATE = 0, L_UP = 36864, L_DOWN = 98304;
constexpr int M_CNT = 0;
constexpr int M_RED = 64;
constexpr int M_BEST = 2432;
constexpr int M_XS = 2560;
constexpr int M_H = 3712;
constexpr int M_RED2 = 4864;
constexpr int M_ACT = 5376;
constexpr int M_RED3 = 5952;
constexpr int M_BESTR = 7040;

#define AUX_X 0
#define AUX_W 0
#define AUX_A 0
#define AUX_B 16
#define AUX_F 0
#define PF_A 1
#define PF_A_NT 0
#define PF_F_NT 0
#define AUX_O 0
#define LPRIO 3
#define DSG 1
#define YPRIO 0
#define PFF_S1 (2 + (j & 3))
#define PFF_S2 (7 + (j & 3))
#define PFF_S3 13
#define SPOLL 1
#define ROT2 0
#define ROT1 0
#define PF_1 1
#define PF_1_NT 0
#define F_PACE 30
constexpr unsigned SPIN_CAP = 1u << 17;

__device__ __forceinline__ float bflo(unsigned u) { return __uint_as_float(u << 16); }
__device__ __forceinline__ float bfhi(unsigned u) { return __uint_as_float(u & 0xffff0000u); }
__device__ __forceinline__ unsigned pk2(float a, float b) { bf2 p = {(__bf16)a, (__bf16)b}; return __builtin_bit_cast(unsigned, p); }
__device__ __forceinline__ float rbf(float f) { return bflo(pk2(f, f)); }
__device__ __forceinline__ f32x4 mfma16(v4u a, v4u b, f32x4 c) {
  return __builtin_amdgcn_mfma_f32_16x16x32_bf16(__builtin_bit_cast(bf16x8, a), __builtin_bit_cast(bf16x8, b), c, 0, 0, 0);
}
__device__ __forceinline__ float dot2(unsigned a, unsigned b, float c) {
  return __builtin_amdgcn_fdot2_f32_bf16(__builtin_bit_cast(bf2, a), __builtin_bit_cast(bf2, b), c, false);
}
#define WAITV(n) asm volatile("s_waitcnt vmcnt(" #n ")" ::: "memory")
#define WAITL0() __builtin_amdgcn_s_waitcnt(0xC07F)
#define BAR() do { asm volatile("" ::: "memory"); __builtin_amdgcn_s_barrier(); asm volatile("" ::: "memory"); } while (0)
#define LBAR() do { WAITL0(); BAR(); } while (0)
#define SCHED() __builtin_amdgcn_sched_barrier(0)
#define GLDS(rsrc, lds, voff, soff, aux) __builtin_amdgcn_raw_ptr_buffer_load_lds(rsrc, (LAS void*)(lds), 16, voff, soff, 0, aux)
#define MKRSRC(p, n) __builtin_amdgcn_make_buffer_rsrc((void*)(p), 0, (n), 0x00020000)
template <int AUX> __device__ __forceinline__ void bstore128(v4u v, __amdgpu_buffer_rsrc_t r, int voff, int soff) {
  asm volatile("" : "+v"(v));
  __builtin_amdgcn_raw_buffer_store_b128(v, r, voff, soff, AUX);
  asm volatile("s_nop 1" :: "v"(v) : "memory");
}
template <int CTRL> __device__ __forceinline__ unsigned dppu(unsigned v) { return (unsigned)__builtin_amdgcn_update_dpp(0, (int)v, CTRL, 0xf, 0xf, true); }
template <int CTRL> __device__ __forceinline__ float dppf(float v) { return __uint_as_float(dppu<CTRL>(__float_as_uint(v))); }
template <int N> __device__ __forceinline__ void interleave_mfma_ds() {
#pragma unroll
  for (int i = 0; i < N; i += DSG) { __builtin_amdgcn_sched_group_barrier(0x8, 1, 0); __builtin_amdgcn_sched_group_barrier(0x100, DSG, 0); }
}
__device__ __forceinline__ float addswap32(float a, float b) {
  auto r = __builtin_amdgcn_permlane32_swap(__float_as_uint(a), __float_as_uint(b), false, false);
  return __uint_as_float(r[0]) + __uint_as_float(r[1]);
}
__device__ __forceinline__ float addswap16(float a, float b) {
  auto r = __builtin_amdgcn_permlane16_swap(__float_as_uint(a), __float_as_uint(b), false, false);
  return __uint_as_float(r[0]) + __uint_as_float(r[1]);
}
__device__ __forceinline__ v4u trfrag(LAS char* p0, LAS char* p1) {
  const v2u a = __builtin_bit_cast(v2u, __builtin_amdgcn_ds_read_tr16_b64_v4i16((LAS s16x4*)p0));
  const v2u b = __builtin_bit_cast(v2u, __builtin_amdgcn_ds_read_tr16_b64_v4i16((LAS s16x4*)p1));
  return v4u{a[0], a[1], b[0], b[1]};
}
__device__ __forceinline__ u64 get_tag(const char* ws) {
  u64 e;
  asm volatile("s_load_dwordx2 %0, %1, 0x0\n\ts_waitcnt lgkmcnt(0)" : "=&s"(e) : "s"(ws) : "memory");
  return e + 1;
}
typedef u64 u64x8 __attribute__((ext_vector_type(8)));
__device__ __forceinline__ unsigned spoll_flags16(const char* base, u64 tag) {
  u64x8 f0, f1;
  asm volatile("s_load_dwordx16 %0, %2, 0x0 glc\n\ts_load_dwordx16 %1, %2, 0x40 glc\n\ts_waitcnt lgkmcnt(0)" : "=&s"(f0), "=&s"(f1) : "s"(base) : "memory");
  unsigned m = 0;
#pragma unroll
  for (int i = 0; i < 8; ++i) m |= ((f0[i] == tag ? 1u : 0u) << i) | ((f1[i] == tag ? 1u : 0u) << (8 + i));
  return m;
}
__device__ __forceinline__ unsigned poll_flags16(GAS char* base, int lane, u64 tag) {
  const u64 v = __hip_atomic_load((GAS u64*)(base + (lane & 15) * 8), RLX_AGENT);
  return (unsigned)__builtin_amdgcn_ballot_w64(v == tag);
}
__device__ __forceinline__ unsigned poll_step(char* base, int lane, u64 tag, unsigned spins) {
  if (SPOLL && (spins & 31u) != 31u) return spoll_flags16(base, tag);
  return poll_flags16((GAS char*)base, lane, tag) & 0xffffu;
}
template <bool SLEEP> __device__ __forceinline__ void lds_wait_ge(LAS unsigned* cnt, unsigned target) {
#pragma unroll 1
  for (unsigned spins = 0; spins < (SPIN_CAP << 4); ++spins) {
    const unsigned v = __hip_atomic_load(cnt, __ATOMIC_RELAXED, __HIP_MEMORY_SCOPE_WORKGROUP);
    if (__builtin_amdgcn_readfirstlane(v) >= target) break;
    if (SLEEP) __builtin_amdgcn_s_sleep(1);
  }
  asm volatile("" ::: "memory");
}
__device__ __forceinline__ void lds_arrive(LAS unsigned* cnt, int lane) {
  asm volatile("" ::: "memory");
  WAITL0();
  if (lane == 0) __hip_atomic_fetch_add(cnt, 1u, __ATOMIC_RELAXED, __HIP_MEMORY_SCOPE_WORKGROUP);
}
#define PFLOAD(d, vo, rs, so, MODS) asm volatile("s_nop 4\n\tbuffer_load_dword %0, %1, %2, %3 offen" MODS : "+v"(d) : "v"(vo), "s"(rs), "s"(so) : "memory")
__device__ __forceinline__ void publish(LAS unsigned* cnt, GAS char* flag, int stride, int ncons, int lane, u64 tag) {
  if (lane == 0) __hip_atomic_fetch_add(cnt + 6, 1u, __ATOMIC_RELAXED, __HIP_MEMORY_SCOPE_WORKGROUP);
  WAITV(0);
  unsigned old = 0;
  if (lane == 0) old = __hip_atomic_fetch_add(cnt, 1u, __ATOMIC_RELAXED, __HIP_MEMORY_SCOPE_WORKGROUP);
  if (__builtin_amdgcn_readfirstlane(old) == 7u && lane < ncons) __hip_atomic_store((GAS u64*)(flag + lane * stride), tag, RLX_AGENT);
}

template <int MH, int GRP>
__device__ __forceinline__ void p1_mma(LAS char* L, int lane, __amdgpu_buffer_rsrc_t rws, int bt_off) {
  constexpr int NT = GRP == 0 ? 3 : 2, N0 = GRP == 0 ? 0 : 1 + 2 * GRP;
  const int r15 = lane & 15, g = lane >> 4;
  const int off0 = r15 * 128 + ((g ^ (r15 >> 1)) * 16), off1 = off0 ^ 64;
  const v4u ones = {0x3f803f80u, 0x3f803f80u, 0x3f803f80u, 0x3f803f80u};
  f32x4 acc[2][NT], sa[2], sw[NT];
#pragma unroll
  for (int a = 0; a < 2; ++a) {
    sa[a] = f32x4{0.f, 0.f, 0.f, 0.f};
#pragma unroll
    for (int b = 0; b < NT; ++b) acc[a][b] = f32x4{0.f, 0.f, 0.f, 0.f};
  }
#pragma unroll
  for (int b = 0; b < NT; ++b) sw[b] = f32x4{0.f, 0.f, 0.f, 0.f};
  v4u xf[2][2], wf[2][NT];
  auto rd = [&](int buf, int sbase) {
    LAS char* sb = L + sbase + (buf ? off1 : off0);
#pragma unroll
    for (int a = 0; a < 2; ++a) xf[buf][a] = *(LAS v4u*)(sb + MH * 4096 + a * 2048);
#pragma unroll
    for (int b = 0; b < NT; ++b) wf[buf][b] = *(LAS v4u*)(sb + 8192 + (N0 + b) * 2048);
  };
  auto mm = [&](int buf) {
#pragma unroll
    for (int b = 0; b < NT; ++b)
#pragma unroll
      for (int a = 0; a < 2; ++a) acc[a][b] = mfma16(xf[buf][a], wf[buf][b], acc[a][b]);
    if (GRP == 1) {
#pragma unroll
      for (int a = 0; a < 2; ++a) sa[a] = mfma16(xf[buf][a], ones, sa[a]);
    }
    if (GRP == 3) {
#pragma unroll
      for (int a = 0; a < 2; ++a) sa[a] = mfma16(xf[buf][a], xf[buf][a], sa[a]);
    }
    if (buf == MH) {
#pragma unroll
      for (int b = 0; b < NT; ++b) sw[b] = mfma16(ones, wf[buf][b], sw[b]);
    }
  };
  BAR();
  rd(0, 0);
  int sbase = 0;
#pragma unroll 1
  for (int i = 0; i < 9; ++i) {
    rd(1, sbase);
    mm(0);
    interleave_mfma_ds<2 + NT>();
    SCHED();
    sbase = sbase == 2 * P1_ST ? 0 : sbase + P1_ST;
    LBAR();
    rd(0, sbase);
    mm(1);
    interleave_mfma_ds<2 + NT>();
    SCHED();
  }
  LAS float* st = (LAS float*)(L + L_STAT);
  if (GRP == 1) {
    if (r15 == 0) {
#pragma unroll
      for (int a = 0; a < 2; ++a) *(LAS f32x4*)(st + MH * 32 + a * 16 + g * 4) = sa[a];
    }
  }
  if (GRP == 3) {
    if ((r15 >> 2) == g) {
#pragma unroll
      for (int a = 0; a < 2; ++a) {
        const int k = r15 & 3;
        st[64 + MH * 32 + a * 16 + r15] = k == 0 ? sa[a][0] : (k == 1 ? sa[a][1] : (k == 2 ? sa[a][2] : sa[a][3]));
      }
    }
  }
  if (g == 0) {
#pragma unroll
    for (int b = 0; b < NT; ++b) st[128 + MH * 144 + (N0 + b) * 16 + r15] = sw[b][0];
  }
  LBAR();
  float wsum[NT];
#pragma unroll
  for (int b = 0; b < NT; ++b) wsum[b] = sw[b][0] + st[128 + (MH ^ 1) * 144 + (N0 + b) * 16 + r15];
  {
    const int row = MH * 32 + (g >> 1) * 16 + r15;
    const float mean = st[row] * (1.0f / D);
    const float var = fmaxf(st[64 + row] * (1.0f / D) - mean * mean, 0.f);
    const float sd = rbf(__builtin_amdgcn_sqrtf(rbf(rbf(var) + 1e-5f)));
    st[512 + (GRP * 2 + MH) * 64 + lane] = (g & 1) ? __builtin_amdgcn_rcpf(sd) : rbf(mean);
  }
  f32x4 mu[2], ri[2];
#pragma unroll
  for (int a = 0; a < 2; ++a) {
    mu[a] = *(LAS f32x4*)(st + 512 + (GRP * 2 + MH) * 64 + a * 32 + g * 4);
    ri[a] = *(LAS f32x4*)(st + 512 + (GRP * 2 + MH) * 64 + a * 32 + 16 + g * 4);
  }
  const int gp = ((g & 1) << 1) | (g >> 1);
#pragma unroll
  for (int b = 0; b < NT; ++b) {
    unsigned p[2][2];
#pragma unroll
    for (int a = 0; a < 2; ++a) {
      float v[4];
#pragma unroll
      for (int i = 0; i < 4; ++i) v[i] = (acc[a][b][i] - mu[a][i] * wsum[b]) * ri[a][i];
      p[a][0] = pk2(v[0], v[1]); p[a][1] = pk2(v[2], v[3]);
    }
    auto r0 = __builtin_amdgcn_permlane16_swap(p[0][0], p[1][0], false, false);
    auto r1 = __builtin_amdgcn_permlane16_swap(p[0][1], p[1][1], false, false);
    v4u o = {r0[0], r1[0], r0[1], r1[1]};
    bstore128<16>(o, rws, (gp * 16 + r15) * 16, bt_off + MH * 9216 + (N0 + b) * 1024);
  }
}

template <int NT>
__device__ __forceinline__ void p2_mma(LAS char* L, int lane, int m, __amdgpu_buffer_rsrc_t rws, int p_off) {
  constexpr int N0 = NT == 5 ? 0 : 5;
  const int r15 = lane & 15, g = lane >> 4;
  const int off0 = P2_A + m * 4096 + r15 * 128 + ((g ^ (r15 >> 1)) * 16), off1 = off0 ^ 64;
  const int boff = N0 * 1024 + lane * 16;
  f32x4 acc[2][NT];
#pragma unroll
  for (int a = 0; a < 2; ++a)
#pragma unroll
    for (int b = 0; b < NT; ++b) acc[a][b] = f32x4{0.f, 0.f, 0.f, 0.f};
  v4u af[2][2], bf[2][NT];
  auto rd = [&](int buf, LAS char* pa, LAS char* pb) {
#pragma unroll
    for (int a = 0; a < 2; ++a) af[buf][a] = *(LAS v4u*)(pa + a * 2048);
#pragma unroll
    for (int b = 0; b < NT; ++b) bf[buf][b] = *(LAS v4u*)(pb + buf * 9216 + b * 1024);
  };
  auto mm = [&](int buf) {
#pragma unroll
    for (int b = 0; b < NT; ++b)
#pragma unroll
      for (int a = 0; a < 2; ++a) acc[a][b] = mfma16(bf[buf][b], af[buf][a], acc[a][b]);
  };
  BAR();
  LAS char* pa0 = L + off0; LAS char* pa1 = L + off1; LAS char* pb = L + boff;
  rd(0, pa0, pb);
  int sa = 0, sb = 0;
#pragma unroll 1
  for (int i = 0; i < 16; ++i) {
    rd(1, pa1, pb);
    mm(0);
    interleave_mfma_ds<2 + NT>();
    sa = (sa + 16384) & 65535;
    sb = sb == 2 * P2_BST ? 0 : sb + P2_BST;
    pa0 = L + sa + off0; pa1 = L + sa + off1; pb = L + sb + boff;
    asm volatile("" : "+v"(pa0), "+v"(pa1), "+v"(pb));
    SCHED();
    LBAR();
    rd(0, pa0, pb);
    mm(1);
    interleave_mfma_ds<2 + NT>();
    SCHED();
  }
#pragma unroll
  for (int b = 0; b < NT; ++b) {
    auto r0 = __builtin_amdgcn_permlane16_swap(pk2(acc[0][b][0], acc[0][b][1]), pk2(acc[1][b][0], acc[1][b][1]), false, false);
    auto r1 = __builtin_amdgcn_permlane16_swap(pk2(acc[0][b][2], acc[0][b][3]), pk2(acc[1][b][2], acc[1][b][3]), false, false);
    v4u o = {r0[0], r1[0], r0[1], r1[1]};
    bstore128<16>(o, rws, (((g & 1) * 2 + (r15 >> 3)) * 36) * 256 + (r15 & 7) * 32 + (g >> 1) * 16,
                                           p_off + (m * 4 * 36 + N0 + b) * 256);
  }
}

__device__ __forceinline__ unsigned rkey(float r, int row) {
  const unsigned u = pk2(r, r) & 0xffffu;
  unsigned k = (u & 0x8000u) ? (~u & 0xffffu) : (u | 0x8000u);
  if ((u & 0x7fffu) > 0x7f80u) k = 0;
  return (k << 16) | (unsigned)(0xffff - row);
}
#define MAXSEL(CTRL) { const unsigned k2 = dppu<CTRL>(key), s2 = dppu<CTRL>(stv); if (k2 > key) { key = k2; stv = s2; } }

__global__ void __launch_bounds__(832, 4)
mk(const u16* __restrict__ x, const u16* __restrict__ wsr, const u16* __restrict__ wsi, const u16* __restrict__ wdr, const u16* __restrict__ wdi,
   const u16* __restrict__ wr, const u16* __restrict__ wg, const u16* __restrict__ wu, const u16* __restrict__ wd, u16* __restrict__ out, char* ws) {
  __shared__ __attribute__((aligned(16))) char smem[163840];
  LAS char* L = (LAS char*)smem;
  const int lane = threadIdx.x & 63, w = __builtin_amdgcn_readfirstlane(threadIdx.x >> 6);
  const int xcd = blockIdx.x & 7, j = blockIdx.x >> 3;
  const int h = xcd >> 2, ct = j & 3, st = (xcd & 3) * 8 + (j >> 2);
  const int kq = xcd >> 1, khalf = kq & 1, rt = (xcd & 1) * 8 + (j >> 2);
  const bool ffn = j < 18;
  const int fs = xcd * 18 + j;
  GAS char* gws = (GAS char*)ws;
  LAS unsigned* cnts = (LAS unsigned*)(L + L_MISC + M_CNT);

  if (w < 4) {
    const int l = w;
    const int rl = lane >> 3;
    {
      const __amdgpu_buffer_rsrc_t rw = MKRSRC(h ? wdi : wdr, D * D * 2);
      const __amdgpu_buffer_rsrc_t rx = MKRSRC(x, S * D * 2);
      const int sw0 = ((lane & 7) ^ ((rl >> 1) & 7)) * 16, sw1 = ((lane & 7) ^ (((rl + 8) >> 1) & 7)) * 16;
      const int v_even = rl * 1152 + sw0, v_odd = rl * 1152 + sw1;
      const int v_a = l == 1 ? v_odd : v_even, v_b = l == 1 ? v_even : v_odd;
      const int wp0 = l < 2 ? 5 * l : 4 * l + 2;
      const int swb = (ct * 144 + wp0 * 8) * 1152, sxb = (st * 64 + l * 16) * 1152;
      auto stage1 = [&](int q, int slot) {
        LAS char* dst = L + slot * P1_ST;
#pragma unroll
        for (int p = 0; p < 4; ++p) {
          if (p < 2) GLDS(rx, dst + l * 2048 + p * 1024, (p & 1) ? v_odd : v_even, sxb + p * 8 * 1152 + q * 128, AUX_X);
          GLDS(rw, dst + 8192 + (wp0 + p) * 1024, (p & 1) ? v_b : v_a, swb + p * 8 * 1152 + q * 128, AUX_W);
        }
        if (l < 2) GLDS(rw, dst + 8192 + (wp0 + 4) * 1024, v_a, swb + 4 * 8 * 1152 + q * 128, AUX_W);
      };
      const int rot2 = ROT2 ? 2 * (j >> 2) : 0;
      int q1 = ROT1 ? (j >> 2) + 2 * ct : 0;
      q1 = q1 >= 9 ? q1 - 9 : q1;
      auto nextq = [&]() { const int q = q1; q1 = q1 == 8 ? 0 : q1 + 1; return q; };
      stage1(nextq(), 0); stage1(nextq(), 1);
      const u64 stag = get_tag(ws + WS_EPOCH);
      WAITV(6);
      BAR();
      stage1(nextq(), 2);
      int slot = 0;
#pragma unroll 1
      for (int i = 0; i < 6; ++i) {
        WAITV(6);
        BAR();
        stage1(nextq(), slot);
        slot = slot == 2 ? 0 : slot + 1;
      }
      WAITV(6); BAR();
      WAITV(0); BAR();
      BAR();
      BAR();
      if (l < 2) {
        const __amdgpu_buffer_rsrc_t ra = MKRSRC(h ? wsi : wsr, S * S * 2);
        const int va_even = rl * 4096 + sw0, va_odd = rl * 4096 + sw1;
        const int sab = (rt * 128 + l * 64) * 4096 + khalf * 2048;
        auto stageA = [&](int i) {
          LAS char* dst = L + P2_A + (i & 3) * 16384 + l * 8192;
          const int kl = (i + rot2) & 15;
#pragma unroll
          for (int p = 0; p < 8; ++p) GLDS(ra, dst + p * 1024, (p & 1) ? va_odd : va_even, sab + p * 8 * 4096 + kl * 128, AUX_A);
        };
        lds_wait_ge<true>(cnts + 5, 1);
        stageA(0); stageA(1);
        WAITV(8);
        BAR();
        stageA(2); stageA(3);
#pragma unroll 1
        for (int i = 0; i < 16; ++i) {
          if (i <= 12) WAITV(16);
          else if (i == 13) WAITV(8);
          else WAITV(0);
          BAR();
          if (i + 4 < 16) stageA(i + 4);
        }
      } else {
        char* fbase = ws + WS_F1 + (rt * 16 + kq * 4 + ct) * 128;
        unsigned mask = 0, spins = 0;
        auto ensure = [&](int i) {
          const unsigned bit = 1u << ((i + rot2) & 15);
#pragma unroll 1
          while (!(mask & bit) && spins < SPIN_CAP) { mask = poll_step(fbase, lane, stag, spins); ++spins; }
        };
        const __amdgpu_buffer_rsrc_t rb = MKRSRC(ws + WS_BT, BT_BYTES);
        const int sbb = ((h * 4 + ct) * 64 + khalf * 32 + (l - 2)) * 9216;
        auto stageB = [&](int i, int sl) {
          LAS char* dst = L + sl * P2_BST + (l - 2) * 9216;
          const int kl = (i + rot2) & 15;
#pragma unroll
          for (int p = 0; p < 3; ++p) {
            __builtin_amdgcn_raw_ptr_buffer_load_lds(rb, (LAS void*)(dst + p * 3072), 16, lane * 16, sbb + kl * 18432 + p * 3072, 0, AUX_B);
            __builtin_amdgcn_raw_ptr_buffer_load_lds(rb, (LAS void*)(dst + p * 3072), 16, lane * 16, sbb + kl * 18432 + p * 3072, 1024, AUX_B);
            __builtin_amdgcn_raw_ptr_buffer_load_lds(rb, (LAS void*)(dst + p * 3072), 16, lane * 16, sbb + kl * 18432 + p * 3072, 2048, AUX_B);
          }
        };
        ensure(0); ensure(1);
        lds_arrive(cnts + 5, lane);
        stageB(0, 0); stageB(1, 1);
        WAITV(9);
        BAR();
        ensure(2);
        stageB(2, 2);
        int sl = 0;
#pragma unroll 1
        for (int i = 0; i < 16; ++i) {
          if (i <= 13) WAITV(9);
          else WAITV(0);
          BAR();
          if (i + 3 < 16) { ensure(i + 3); stageB(i + 3, sl); }
          sl = sl == 2 ? 0 : sl + 1;
        }
      }
    }
    if (ffn) {
      const __amdgpu_buffer_rsrc_t rg = MKRSRC((l & 1) ? wu : wg, D * F * 2);
      const int vg = (lane >> 2) * 9216 + (lane & 3) * 16;
      lds_wait_ge<true>(cnts + 1, 8);
      int p = (fs & 1) * 18 + (l >> 1);
#pragma unroll 2
      for (int i = 0; i < 18; ++i) {
        GLDS(rg, L + (l & 1) * L_UP + p * 1024, vg, p * 16 * 9216 + fs * 64, AUX_F);
        p = p >= 34 ? p - 34 : p + 2;
      }
      WAITV(0);
      BAR();
    }
    return;
  }

  const int t = threadIdx.x - 256;
  const int cw = w - 4;
  const __amdgpu_buffer_rsrc_t rws = MKRSRC(ws, WS_END);
  const int rw = xcd * 14 + (j - 18), rp = rw / 7, rk = rw - rp * 7;
  const int tb0 = rp * 16 + (rk < 2 ? 3 * rk : 2 * rk + 2), nb = rk < 2 ? 3 : 2;
  const int row = (t >> 1) & 7, ech = (t >> 4) * 2 + (t & 1);
  const int xo = row * 1152 + ech * 16;
  const __amdgpu_buffer_rsrc_t rxx = MKRSRC(x, S * D * 2);
  v4u xv[3], wv;
  auto load_x = [&]() {
    if (!ffn) {
#pragma unroll
      for (int q = 0; q < 3; ++q) xv[q] = __builtin_amdgcn_raw_buffer_load_b128(rxx, xo, q < nb ? (tb0 + q) * 9216 : 0x40000000, 0);
      wv = *(const GAS v4u*)((const GAS char*)wr + ech * 16);
    }
  };
  u64 tag;
  if (cw < 8) {
    if (t < 288 && ffn) bstore128<16>(v4u{0u, 0u, 0u, 0u}, rws, t * 16, WS_G + fs * 4608);
    if (t >= 320 && t < 328) cnts[t - 320] = 0u;
    tag = get_tag(ws + WS_EPOCH);
    WAITL0();
    if (YPRIO && cw >= 4) __builtin_amdgcn_s_setprio(1);
    const int bt_off = WS_BT + ((h * 4 + ct) * 64 + st * 2) * 9216;
    switch (cw) {
      case 0: p1_mma<0, 0>(L, lane, rws, bt_off); break;
      case 1: p1_mma<1, 0>(L, lane, rws, bt_off); break;
      case 2: p1_mma<0, 1>(L, lane, rws, bt_off); break;
      case 3: p1_mma<1, 1>(L, lane, rws, bt_off); break;
      case 4: p1_mma<0, 2>(L, lane, rws, bt_off); break;
      case 5: p1_mma<1, 2>(L, lane, rws, bt_off); break;
      case 6: p1_mma<0, 3>(L, lane, rws, bt_off); break;
      default: p1_mma<1, 3>(L, lane, rws, bt_off); break;
    }
    publish(cnts + 0, gws + WS_F1 + ((h * 2 + (st >> 4)) * 4 + ct) * 128 + (st & 15) * 8, 2048, 16, lane, tag);
    load_x();
    const int p_off = WS_P + kq * P_PART + (rt * 16 * 36 + ct * 9) * 256;
    if (cw < 4) p2_mma<5>(L, lane, cw & 3, rws, p_off);
    else p2_mma<4>(L, lane, cw & 3, rws, p_off);
    publish(cnts + 1, gws + WS_F2 + rt * 7 * 128 + (kq * 4 + ct) * 8, 128, 7, lane, tag);
  } else {
    unsigned d0 = 0;
    if (PF_1) {
      const __amdgpu_buffer_rsrc_t rw = MKRSRC(h ? wdi : wdr, D * D * 2);
      const __amdgpu_buffer_rsrc_t rx = MKRSRC(x, S * D * 2);
      const int v54 = lane < 54 ? lane * 128 : 0x40000000, v48 = lane < 48 ? lane * 128 : 0x40000000;
#pragma unroll
      for (int m = 0; m < 3; ++m) {
        if (PF_1_NT) { PFLOAD(d0, v54, rw, (j * 18 + m * 6) * 1152, " nt"); PFLOAD(d0, v48, rx, ((xcd & 3) * 512 + j * 16) * 1152 + m * 6144, " nt"); }
        else { PFLOAD(d0, v54, rw, (j * 18 + m * 6) * 1152, ""); PFLOAD(d0, v48, rx, ((xcd & 3) * 512 + j * 16) * 1152 + m * 6144, ""); }
      }
    }
    tag = get_tag(ws + WS_EPOCH);
    const u64 stag = tag;
    {
      const __amdgpu_buffer_rsrc_t ra = MKRSRC(h ? wsi : wsr, S * S * 2);
      const int voff = (lane >> 1) * 4096 + (lane & 1) * 128;
      const int sb = ((xcd & 1) * 1024 + j * 32) * 4096 + khalf * 2048;
#define PFA(m) do { if (PF_A) { if (PF_A_NT) PFLOAD(d0, voff, ra, sb + (m) * 256, " nt"); else PFLOAD(d0, voff, ra, sb + (m) * 256, ""); } } while (0)
      BAR();
#pragma unroll 1
      for (int i = 0; i < 7; ++i) BAR();
      PFA(0); PFA(1); PFA(2); PFA(3);
      BAR();
      BAR();
      BAR();
      lds_wait_ge<true>(cnts + 6, 8);
      PFA(4); PFA(5); PFA(6); PFA(7);
    }
    load_x();
    {
      const int l9 = lane / 9, lr = lane - l9 * 9;
      const int vgu = lane < 54 ? l9 * 9216 + lr * 128 : 0x40000000;
      const int vdn = lane < 54 ? lane * 128 : 0x40000000;
      const __amdgpu_buffer_rsrc_t rg = MKRSRC(wg, D * F * 2), ru = MKRSRC(wu, D * F * 2), rd = MKRSRC(wd, D * F * 2);
      const int sgu = j * 18 * 9216 + xcd * 1152, sdn = (xcd * 576 + j * 18) * 1152;
#define PFF(vo, rs, so) do { if (PF_F_NT) PFLOAD(d0, vo, rs, so, " nt"); else PFLOAD(d0, vo, rs, so, ""); } while (0)
      const int s1 = PFF_S1, s2 = PFF_S2, s3 = PFF_S3;
      BAR();
#pragma unroll 1
      for (int i = 0; i < 16; ++i) {
        BAR();
        if (PF_F && i == s1) { PFF(vgu, rg, sgu); PFF(vgu, rg, sgu + 6 * 9216); PFF(vgu, rg, sgu + 12 * 9216); }
        if (PF_F && i == s2) { PFF(vgu, ru, sgu); PFF(vgu, ru, sgu + 6 * 9216); PFF(vgu, ru, sgu + 12 * 9216); }
        if (PF_F && i == s3) { PFF(vdn, rd, sdn); PFF(vdn, rd, sdn + 6 * 1152); PFF(vdn, rd, sdn + 12 * 1152); }
      }
    }
    if (ffn) {
      const __amdgpu_buffer_rsrc_t rd = MKRSRC(wd, D * F * 2);
      lds_wait_ge<true>(cnts + 1, 8);
#pragma unroll 2
      for (int p = 0; p < 36; ++p) GLDS(rd, L + L_DOWN + p * 1024, lane * 16, fs * 36864 + p * 1024, AUX_F);
    } else {
      unsigned mask = 0;
#pragma unroll 1
      for (unsigned spins = 0; mask != 0xffffu && spins < SPIN_CAP; ++spins) mask = poll_step(ws + WS_F2 + rw * 128, lane, stag, spins);
    }
    WAITV(0);
    __builtin_amdgcn_s_waitcnt(0x0F70);
    asm volatile("" :: "v"(d0));
  }
  BAR();
  unsigned c_lo_p = 0x3f80u, c_lo_n = 0xbf80u;
  asm volatile("" : "+s"(c_lo_p), "+s"(c_lo_n));
  const unsigned tag32 = (unsigned)tag;
  const __amdgpu_buffer_rsrc_t rout = MKRSRC(out, S * D * 2);
  unsigned key, stv;
  auto gather = [&](LAS unsigned* best, int sleep) {
    if (cw < 2) {
      v4u c;
#pragma unroll 1
      for (unsigned spins = 0; spins < SPIN_CAP; ++spins) {
        asm volatile("" ::: "memory");
        c = __builtin_amdgcn_raw_buffer_load_b128(rws, (t < 112 ? t : 0) * 16, WS_CAND, 16);
        if (__all(c[1] == tag32 && c[3] == tag32)) break;
        if (sleep) __builtin_amdgcn_s_sleep(8);
      }
      key = c[0]; stv = c[2];
      MAXSEL(0xB1); MAXSEL(0x4E); MAXSEL(0x124); MAXSEL(0x128);
      {
        auto k2 = __builtin_amdgcn_permlane16_swap(key, key, false, false); auto s2 = __builtin_amdgcn_permlane16_swap(stv, stv, false, false);
        const bool gt = k2[1] > k2[0]; key = gt ? k2[1] : k2[0]; stv = gt ? s2[1] : s2[0];
      }
      {
        auto k2 = __builtin_amdgcn_permlane32_swap(key, key, false, false); auto s2 = __builtin_amdgcn_permlane32_swap(stv, stv, false, false);
        const bool gt = k2[1] > k2[0]; key = gt ? k2[1] : k2[0]; stv = gt ? s2[1] : s2[0];
      }
      if (lane == 0) *(LAS v2u*)(best + cw * 2) = v2u{key, stv};
    }
    LBAR();
    const v4u b0 = *(LAS v4u*)best;
    key = b0[0]; stv = b0[1];
    if (b0[2] > key) { key = b0[2]; stv = b0[3]; }
  };

  if (!ffn) {
    v4u pp[3][4];
#pragma unroll
    for (int q = 0; q < 3; ++q)
#pragma unroll
      for (int i = 0; i < 4; ++i) pp[q][i] = __builtin_amdgcn_raw_buffer_load_b128(rws, t * 16, q < nb ? WS_P + i * P_PART + (tb0 + q) * 9216 : 0x40000000, 16);
    SCHED();
    float wsm = 0.f;
#pragma unroll
    for (int i = 0; i < 4; ++i) wsm = dot2(wv[i], 0x3f803f80u, wsm);
    LAS float* red = (LAS float*)(L + L_MISC + M_RED);
    v4u hb[3];
#pragma unroll
    for (int q = 0; q < 3; ++q) {
      float s1 = 0.f, s2 = 0.f, s3 = 0.f;
#pragma unroll
      for (int i = 0; i < 4; ++i) {
        float lo = bflo(xv[q][i]), hi = bfhi(xv[q][i]);
        lo = dot2(pp[q][0][i], c_lo_p, lo); hi = dot2(pp[q][0][i], 0x3f800000u, hi);
        lo = dot2(pp[q][1][i], c_lo_p, lo); hi = dot2(pp[q][1][i], 0x3f800000u, hi);
        lo = dot2(pp[q][2][i], c_lo_n, lo); hi = dot2(pp[q][2][i], 0xbf800000u, hi);
        lo = dot2(pp[q][3][i], c_lo_n, lo); hi = dot2(pp[q][3][i], 0xbf800000u, hi);
        hb[q][i] = pk2(lo, hi);
        s1 = dot2(hb[q][i], 0x3f803f80u, s1);
        s2 = dot2(hb[q][i], hb[q][i], s2);
        s3 = dot2(hb[q][i], wv[i], s3);
      }
      float v = addswap16(addswap32(s1, s2), addswap32(s3, wsm));
      v += dppf<0xB1>(v);
      red[(q * 9 + cw) * 64 + lane] = v;
      SCHED();
    }
    LBAR();
    float mb[3], ri[3];
    key = 0; stv = 0;
#pragma unroll
    for (int q = 0; q < 3; ++q) {
      float tot = 0.f;
#pragma unroll
      for (int i = 0; i < 9; ++i) tot += red[(q * 9 + i) * 64 + lane];
      auto ra = __builtin_amdgcn_permlane16_swap(__float_as_uint(tot), __float_as_uint(tot), false, false);
      auto rb = __builtin_amdgcn_permlane32_swap(ra[0], ra[0], false, false);
      auto rc = __builtin_amdgcn_permlane32_swap(ra[1], ra[1], false, false);
      const float mean = __uint_as_float(rb[0]) * (1.0f / D);
      const float var = fmaxf(__uint_as_float(rb[1]) * (1.0f / D) - mean * mean, 0.f);
      mb[q] = rbf(mean);
      const float sd = rbf(__builtin_amdgcn_sqrtf(rbf(rbf(var) + 1e-5f)));
      ri[q] = __builtin_amdgcn_rcpf(sd);
      const float r = (__uint_as_float(rc[0]) - mb[q] * __uint_as_float(rc[1])) * ri[q];
      const unsigned k = rkey(r, (tb0 + q) * 8 + row);
      if (q < nb && k > key) { key = k; stv = pk2(mb[q], sd); }
    }
    MAXSEL(0x4E); MAXSEL(0x124); MAXSEL(0x128);
    if (t == 0) bstore128<16>(v4u{key, tag32, stv, tag32}, rws, 0, WS_CAND + rw * 16);
    const int brow = (int)(0xffffu - (key & 0xffffu));
    v4u ho = {0u, 0u, 0u, 0u};
    bool held = false;
#pragma unroll
    for (int q = 0; q < 3; ++q) {
      v4u o;
#pragma unroll
      for (int i = 0; i < 4; ++i) {
        const unsigned c = pk2(bflo(hb[q][i]) - mb[q], bfhi(hb[q][i]) - mb[q]);
        const unsigned hn = pk2(bflo(c) * ri[q], bfhi(c) * ri[q]);
        o[i] = pk2(bflo(hb[q][i]) + bflo(hn), bfhi(hb[q][i]) + bfhi(hn));
      }
      const bool mine = (tb0 + q) * 8 + row == brow;
      if (mine) { ho = o; held = true; }
      else bstore128<AUX_O>(o, rout, xo, q < nb ? (tb0 + q) * 9216 : 0x40000000);
    }
    gather((LAS unsigned*)(L + L_MISC + M_BESTR), 1);
    if (held && brow != (int)(0xffffu - (key & 0xffffu))) bstore128<AUX_O>(ho, rout, brow * 1152 + ech * 16, 0);
    return;
  }

  const int g = lane >> 4, tq = (lane >> 2) & 3, tp = lane & 3;
  v4u gw[9], dw[4];
  {
    const int tile = cw & 3, kh = (cw >> 2) & 1;
    LAS char* wb = L + (tile >> 1) * L_UP + (kh * 288 + 8 * g + tq) * 64 + (tile & 1) * 32 + tp * 8;
#pragma unroll
    for (int ks = 0; ks < 9; ++ks) gw[ks] = trfrag(wb + ks * 2048, wb + ks * 2048 + 256);
#pragma unroll
    for (int n = 0; n < 4; ++n) {
      LAS char* db = L + L_DOWN + (8 * g + tq) * 1152 + (cw * 4 + n) * 32 + tp * 8;
      dw[n] = trfrag(db, db + 4 * 1152);
    }
  }
  gather((LAS unsigned*)(L + L_MISC + M_BEST), 0);
  const int idx = (int)(0xffffu - (key & 0xffffu)) & (S - 1);
  if (blockIdx.x == 0 && t == 0) __hip_atomic_store((GAS u64*)(gws + WS_EPOCH), tag, RLX_AGENT);
  if (t < 288) {
    const int po = (idx >> 3) * 9216 + (t >> 3) * 256 + (idx & 7) * 32 + (t & 7) * 4;
    unsigned q[4];
#pragma unroll
    for (int i = 0; i < 4; ++i) q[i] = __builtin_amdgcn_raw_buffer_load_b32(rws, po, WS_P + i * P_PART, 16);
    const unsigned xx = __builtin_amdgcn_raw_buffer_load_b32(rxx, idx * 1152 + t * 4, 0, 0);
    float lo = bflo(xx), hi = bfhi(xx);
    lo = dot2(q[0], c_lo_p, lo); hi = dot2(q[0], 0x3f800000u, hi);
    lo = dot2(q[1], c_lo_p, lo); hi = dot2(q[1], 0x3f800000u, hi);
    lo = dot2(q[2], c_lo_n, lo); hi = dot2(q[2], 0xbf800000u, hi);
    lo = dot2(q[3], c_lo_n, lo); hi = dot2(q[3], 0xbf800000u, hi);
    const unsigned hh = pk2(lo, hi);
    const float m2 = bflo(stv), r2 = __builtin_amdgcn_rcpf(bfhi(stv));
    const unsigned c = pk2(bflo(hh) - m2, bfhi(hh) - m2);
    *(LAS unsigned*)(L + L_MISC + M_XS + t * 4) = pk2(bflo(c) * r2, bfhi(c) * r2);
    *(LAS unsigned*)(L + L_MISC + M_H + t * 4) = hh;
  }
  LBAR();
  if (cw < 8) {
    const int tile = cw & 3, kh = cw >> 2;
    LAS char* xb = L + L_MISC + M_XS + (kh * 288 + 8 * g) * 2;
    f32x4 acc0 = {0.f, 0.f, 0.f, 0.f}, acc1 = {0.f, 0.f, 0.f, 0.f};
#pragma unroll
    for (int ks = 0; ks < 9; ++ks) {
      if (ks & 1) acc1 = mfma16(*(LAS v4u*)(xb + ks * 64), gw[ks], acc1);
      else acc0 = mfma16(*(LAS v4u*)(xb + ks * 64), gw[ks], acc0);
    }
    if (lane < 16) ((LAS float*)(L + L_MISC + M_RED2))[kh * 64 + tile * 16 + lane] = acc0[0] + acc1[0];
  }
  LBAR();
  {
    LAS float* r2 = (LAS float*)(L + L_MISC + M_RED2);
    const int f = lane & 31;
    const float gg = rbf(r2[f] + r2[64 + f]), uu = rbf(r2[32 + f] + r2[96 + f]);
    const float sg = rbf(gg * __builtin_amdgcn_rcpf(1.0f + __expf(-gg)));
    if (lane < 32) *(LAS u16*)(L + L_MISC + M_ACT + cw * 64 + f * 2) = (u16)pk2(sg * uu, 0.f);
    const v4u af = *(LAS v4u*)(L + L_MISC + M_ACT + cw * 64 + g * 16);
    f32x4 y[4];
#pragma unroll
    for (int n = 0; n < 4; ++n) y[n] = mfma16(af, dw[n], f32x4{0.f, 0.f, 0.f, 0.f});
    const float yv = g == 0 ? y[0][0] : (g == 1 ? y[1][0] : (g == 2 ? y[2][0] : y[3][0]));
    __builtin_amdgcn_raw_buffer_store_b64(v2u{__float_as_uint(yv), 1u}, rws, t * 8, WS_G + fs * 4608, 16);
  }
  {
    v2u gv;
#pragma unroll 1
    for (unsigned spins = 0; spins < SPIN_CAP; ++spins) {
      asm volatile("" ::: "memory");
      gv = __builtin_amdgcn_raw_buffer_load_b64(rws, (t >> 2) * 4608 + (t & 3) * 8, WS_G + fs * 32, 16);
      if (__all(gv[1] == 1u)) break;
    }
    float v = __uint_as_float(gv[0]);
    v += dppf<0x124>(v); v += dppf<0x128>(v);
    v = addswap16(v, v); v = addswap32(v, v);
    if (lane < 4) ((LAS float*)(L + L_MISC + M_RED3))[cw * 4 + lane] = v;
  }
  LBAR();
  if (t < 4) {
    float y = 0.f;
#pragma unroll
    for (int i = 0; i < 9; ++i) y += ((LAS float*)(L + L_MISC + M_RED3))[i * 4 + t];
    const int e = fs * 4 + t;
    const float hh = bflo(*(LAS u16*)(L + L_MISC + M_H + e * 2)), hn = bflo(*(LAS u16*)(L + L_MISC + M_XS + e * 2));
    const unsigned k16 = key >> 16;
    const float r = bflo((k16 & 0x8000u) ? (k16 & 0x7fffu) : (~k16 & 0xffffu));
    const float sg = rbf(__builtin_amdgcn_rcpf(1.0f + __expf(-r)));
    const float yy = rbf(rbf(y) * sg);
    __builtin_amdgcn_raw_buffer_store_b16((u16)pk2(hh + rbf(hn + yy), 0.f), rout, (idx * D + e) * 2, 0, AUX_O);
  }
}

extern "C" void kernel_launch(void* const* d_in, const int* in_sizes, int n_in, void* d_out, int out_size, void* d_ws, size_t ws_size,
                              hipStream_t stream) {
  mk<<<256, 832, 0, stream>>>((const u16*)d_in[0], (const u16*)d_in[1], (const u16*)d_in[2], (const u16*)d_in[3], (const u16*)d_in[4],
                              (const u16*)d_in[6], (const u16*)d_in[7], (const u16*)d_in[8], (const u16*)d_in[9], (u16*)d_out, (char*)d_ws);
}
```
